```python
import torch

def _softmax(x, axis=-1):
    return torch.softmax(x, dim=axis)

def _gelu(x):
    return torch.nn.functional.gelu(x, approximate='tanh')

def _silu(x):
    return torch.nn.functional.silu(x)

def _sigmoid(x):
    return torch.sigmoid(x)

def _layernorm(x, axis=-1, eps=1e-05):
    m = x.mean(dim=axis, keepdim=True)
    v = x.var(dim=axis, keepdim=True, unbiased=False)
    return (x - m) / torch.sqrt(v + eps)

def _scatter_add(tgt, idx, src, axis=0):
    return tgt.clone().index_add_(axis, idx, src)

def _segment_sum(src, seg, n):
    out = torch.zeros((n,) + src.shape[1:], dtype=src.dtype)
    return out.index_add_(0, seg, src)

def _pad(x, pw):
    flat = tuple((v for pair in reversed(pw) for v in pair))
    return torch.nn.functional.pad(x, flat)
kernel_name = 'adaLN_cond__memory_aug_recurrent_mem_tr__dense_activation_squared_relu__prompt'

def setup_inputs():

    _w = lambda a: (lambda t: t.to(torch.bfloat16) if t.is_floating_point() else t)(torch.as_tensor(a))

    def _A_setup():
        B, S, Dm = (1, 2048, 640)
        H, Dh = (32, 192)
        M, seg = (8, 32)
        seg = min(seg, S)
        S = S // seg * seg
        return {'x': _w((lambda *s: torch.randn(*s, dtype=torch.bfloat16) * 0.05)(B, S, Dm)), 'mem0': _w((lambda *s: torch.randn(*s, dtype=torch.bfloat16) * 0.4)(B, M, Dm)), 'Wq': _w((lambda *s: torch.randn(*s, dtype=torch.bfloat16) * 3.2)(Dm, H, Dh) * Dm ** (-0.5)), 'Wk': _w((lambda *s: torch.randn(*s, dtype=torch.bfloat16) * 3.2)(Dm, H, Dh) * Dm ** (-0.5)), 'Wv': _w((lambda *s: torch.randn(*s, dtype=torch.bfloat16) * 0.4)(Dm, H, Dh) * Dm ** (-0.5)), 'Wo': _w((lambda *s: torch.randn(*s, dtype=torch.bfloat16) * 0.4)(H, Dh, Dm) * (H * Dh) ** (-0.5)), 'seg_len': seg}
    _A_in = _A_setup()

    def _B_setup():
        B, S, D, F = (1, 2048, 640, 1536)
        return {'x': _w((lambda *s: torch.randn(*s, dtype=torch.bfloat16) * 0.05)(B, S, D)), 'w1': _w((lambda *s: torch.randn(*s, dtype=torch.bfloat16) * 0.05)(D, F)), 'b1': _w((lambda *s: torch.randn(*s, dtype=torch.bfloat16) * 0.4)(F)), 'w2': _w((lambda *s: torch.randn(*s, dtype=torch.bfloat16) * 0.05)(F, D)), 'b2': _w((lambda *s: torch.randn(*s, dtype=torch.bfloat16) * 3.2)(D))}
    _B_in = _B_setup()
    return {'A_x': _A_in['x'], 'A_mem0': _A_in['mem0'], 'A_Wq': _A_in['Wq'], 'A_Wk': _A_in['Wk'], 'A_Wv': _A_in['Wv'], 'A_Wo': _A_in['Wo'], 'A_seg_len': _A_in['seg_len'], 'B_x': _B_in['x'], 'B_w1': _B_in['w1'], 'B_b1': _B_in['b1'], 'B_w2': _B_in['w2'], 'B_b2': _B_in['b2'], 'c': _w((lambda *s: torch.randn(*s, dtype=torch.bfloat16) * 0.1)(1, 640) * 8.0), 'w_mod': _w((lambda *s: torch.randn(*s, dtype=torch.bfloat16) * 0.1)(640, 6 * 640) * 640 ** (-0.5) * 8.0)}

def reference(A_x, A_mem0, A_Wq, A_Wk, A_Wv, A_Wo, A_seg_len, B_x, B_w1, B_b1, B_w2, B_b2, c, w_mod):

    def _A_ref(x, mem0, Wq, Wk, Wv, Wo, seg_len):
        B, S, Dm = x.shape
        H, Dh = (Wq.shape[1], Wq.shape[2])
        M = mem0.shape[1]
        scale = Dh ** (-0.5)
        n_seg = S // seg_len
        L = M + seg_len
        idx = torch.arange(L)
        mask = (idx[:, None] < M) | (idx[:, None] >= idx[None, :])
        mem = mem0
        outs = []
        for s in range(n_seg):
            xs = x[:, s * seg_len:(s + 1) * seg_len, :].clone()
            h = torch.concatenate([mem, xs], axis=1)
            q = torch.einsum('btd,dhe->bhte', h, Wq)
            k = torch.einsum('btd,dhe->bhte', h, Wk)
            v = torch.einsum('btd,dhe->bhte', h, Wv)
            scores = torch.einsum('bhqe,bhke->bhqk', q, k) * scale
            scores = torch.where(mask[None, None], scores, -1000000000.0)
            p = _softmax(scores, axis=-1)
            o = torch.einsum('bhqk,bhke->bhqe', p, v)
            o = torch.einsum('bhte,hed->btd', o, Wo)
            mem = o[:, :M, :].clone()
            outs.append(o[:, M:, :])
        return torch.concatenate(outs, axis=1)

    def _B_ref(x, w1, b1, w2, b2):
        h = torch.einsum('bsd,df->bsf', x, w1) + b1
        zero = torch.zeros_like(h)
        r = torch.where(h > zero, h, zero)
        h = r * r
        out = torch.einsum('bsf,fd->bsd', h, w2) + b2
        return out
    x = A_x
    mod = (_silu(c) @ w_mod).reshape(c.shape[0], 6, 1, -1)
    sa, ca, ga, sb, cb, gb = (mod[:, 0].clone(), mod[:, 1].clone(), mod[:, 2].clone(), mod[:, 3].clone(), mod[:, 4].clone(), mod[:, 5].clone())
    h = _layernorm(x, axis=-1) * (1 + ca) + sa
    x = x + ga * _A_ref(h, A_mem0, A_Wq, A_Wk, A_Wv, A_Wo, A_seg_len)
    h = _layernorm(x, axis=-1) * (1 + cb) + sb
    x = x + gb * _B_ref(h, B_w1, B_b1, B_w2, B_b2)
    return x
```

```cpp
#include <hip/hip_runtime.h>
#include <hip/hip_bf16.h>
#include <stdint.h>

typedef unsigned short u16;
typedef __bf16 bf16x8 __attribute__((ext_vector_type(8)));
typedef float f32x4 __attribute__((ext_vector_type(4)));

__device__ __forceinline__ float bf2f(u16 v) { return __uint_as_float(((unsigned)v) << 16); }
__device__ __forceinline__ u16 f2bf(float f) { __bf16 b = (__bf16)f; return __builtin_bit_cast(u16, b); }
__device__ __forceinline__ float rbf(float f) { return bf2f(f2bf(f)); }

constexpr int S = 2048, DM = 640, H = 32, DH = 192, HD = H * DH, MEM = 8, SEG = 32, NSEG = 64, L = 40, FF = 1536;

__global__ void k_mod(const u16* c, const u16* wmod, float* mod) {
  __shared__ float sc[DM];
  for (int i = threadIdx.x; i < DM; i += blockDim.x) { float v = bf2f(c[i]); sc[i] = rbf(v / (1.f + __expf(-v))); }
  __syncthreads();
  int n = blockIdx.x * blockDim.x + threadIdx.x;
  if (n >= 6 * DM) return;
  float acc = 0.f;
  for (int k = 0; k < DM; ++k) acc += sc[k] * bf2f(wmod[(size_t)k * 6 * DM + n]);
  mod[n] = rbf(acc);
}

__global__ void k_ln(const u16* x, const float* cc, const float* ss, u16* h) {
  int row = blockIdx.x * 4 + (threadIdx.x >> 6), lane = threadIdx.x & 63;
  float v[10]; float s = 0.f;
  for (int i = 0; i < 10; ++i) { v[i] = bf2f(x[(size_t)row * DM + lane + 64 * i]); s += v[i]; }
  for (int o = 32; o; o >>= 1) s += __shfl_xor(s, o);
  float mean = s / DM, q = 0.f;
  for (int i = 0; i < 10; ++i) { float d = v[i] - mean; q += d * d; }
  for (int o = 32; o; o >>= 1) q += __shfl_xor(q, o);
  float m = rbf(mean), var = rbf(q / DM);
  float sd = rbf(sqrtf(rbf(var + 1e-5f)));
  for (int i = 0; i < 10; ++i) {
    int col = lane + 64 * i;
    float n = rbf(rbf(v[i] - m) / sd);
    h[(size_t)row * DM + col] = f2bf(rbf(n * rbf(1.f + cc[col])) + ss[col]);
  }
}

template <int EPI>
__global__ void __launch_bounds__(256) k_gemm(const u16* A, int lda, const u16* B, int ldb, u16* C, int ldc, int M, int N, int K,
                                              const u16* bias, const float* gate, const u16* res) {
  __shared__ u16 As[64][40];
  __shared__ u16 Bs[64][40];
  const int tid = threadIdx.x, lane = tid & 63, w = tid >> 6;
  const int m0 = blockIdx.y * 64, n0 = blockIdx.x * 64;
  const int wm = (w >> 1) * 32, wn = (w & 1) * 32;
  f32x4 acc[2][2] = {};
  for (int k0 = 0; k0 < K; k0 += 32) {
    { int r = tid >> 2, c = (tid & 3) * 8;
      for (int j = 0; j < 8; ++j) As[r][c + j] = (m0 + r < M) ? A[(size_t)(m0 + r) * lda + k0 + c + j] : (u16)0; }
    { int k = tid >> 3, c = (tid & 7) * 8;
      for (int j = 0; j < 8; ++j) Bs[c + j][k] = B[(size_t)(k0 + k) * ldb + n0 + c + j]; }
    __syncthreads();
    bf16x8 a[2], b[2];
    for (int i = 0; i < 2; ++i) {
      a[i] = *(const bf16x8*)&As[wm + i * 16 + (lane & 15)][(lane >> 4) * 8];
      b[i] = *(const bf16x8*)&Bs[wn + i * 16 + (lane & 15)][(lane >> 4) * 8];
    }
    for (int i = 0; i < 2; ++i) for (int j = 0; j < 2; ++j) acc[i][j] = __builtin_amdgcn_mfma_f32_16x16x32_bf16(a[i], b[j], acc[i][j], 0, 0, 0);
    __syncthreads();
  }
  for (int i = 0; i < 2; ++i) for (int j = 0; j < 2; ++j) for (int r = 0; r < 4; ++r) {
    int m = m0 + wm + i * 16 + (lane >> 4) * 4 + r, n = n0 + wn + j * 16 + (lane & 15);
    if (m >= M) continue;
    float v = rbf(acc[i][j][r]);
    if (EPI == 1) { v = rbf(v + bf2f(bias[n])); v = v > 0.f ? v : 0.f; v = v * v; }
    if (EPI == 2) { v = rbf(v + bf2f(bias[n])); v = bf2f(res[(size_t)m * ldc + n]) + rbf(gate[n] * v); }
    C[(size_t)m * ldc + n] = f2bf(v);
  }
}

__global__ void __launch_bounds__(256) k_attn(const u16* qm, const u16* km, const u16* vm, const u16* Qx, const u16* Kx, const u16* Vx, int s, u16* o) {
  __shared__ float q[L][DH + 1], k[L][DH + 1], v[L][DH + 1], p[L][L + 1];
  const int h = blockIdx.x, tid = threadIdx.x;
  for (int i = tid; i < L * DH; i += 256) {
    int r = i / DH, e = i % DH;
    size_t off = r < MEM ? (size_t)r * HD + h * DH + e : (size_t)(s * SEG + r - MEM) * HD + h * DH + e;
    q[r][e] = bf2f(r < MEM ? qm[off] : Qx[off]); k[r][e] = bf2f(r < MEM ? km[off] : Kx[off]); v[r][e] = bf2f(r < MEM ? vm[off] : Vx[off]);
  }
  __syncthreads();
  const float scale = 0.07216878364870322f;
  for (int i = tid; i < L * L; i += 256) {
    int a = i / L, b = i % L; float acc = 0.f;
    for (int e = 0; e < DH; ++e) acc += q[a][e] * k[b][e];
    acc = rbf(rbf(acc) * scale);
    p[a][b] = (a < MEM || a >= b) ? acc : -1e9f;
  }
  __syncthreads();
  if (tid < L) {
    float m = -3e38f; for (int b = 0; b < L; ++b) m = fmaxf(m, p[tid][b]);
    float sum = 0.f; for (int b = 0; b < L; ++b) { float e = __expf(p[tid][b] - m); p[tid][b] = e; sum += e; }
    for (int b = 0; b < L; ++b) p[tid][b] = rbf(p[tid][b] / sum);
  }
  __syncthreads();
  for (int i = tid; i < L * DH; i += 256) {
    int r = i / DH, e = i % DH; float acc = 0.f;
    for (int b = 0; b < L; ++b) acc += p[r][b] * v[b][e];
    o[(size_t)r * HD + h * DH + e] = f2bf(acc);
  }
}

__global__ void k_scatter(const u16* oo, u16* mem, const u16* x, const float* ga, u16* x1, int s) {
  int i = blockIdx.x * blockDim.x + threadIdx.x;
  if (i >= L * DM) return;
  int r = i / DM, c = i % DM;
  if (r < MEM) mem[i] = oo[i];
  else { size_t off = (size_t)(s * SEG + r - MEM) * DM + c; x1[off] = f2bf(bf2f(x[off]) + rbf(ga[c] * bf2f(oo[i]))); }
}

extern "C" void kernel_launch(void* const* d_in, const int* in_sizes, int n_in, void* d_out, int out_size, void* d_ws, size_t ws_size, hipStream_t stream) {
  const u16 *x = (const u16*)d_in[0], *mem0 = (const u16*)d_in[1], *Wq = (const u16*)d_in[2], *Wk = (const u16*)d_in[3], *Wv = (const u16*)d_in[4], *Wo = (const u16*)d_in[5];
  const u16 *w1 = (const u16*)d_in[8], *b1 = (const u16*)d_in[9], *w2 = (const u16*)d_in[10], *b2 = (const u16*)d_in[11], *c = (const u16*)d_in[12], *wmod = (const u16*)d_in[13];
  char* ws = (char*)d_ws; size_t off = 0;
  auto alloc = [&](size_t bytes) { void* p = ws + off; off += (bytes + 255) & ~(size_t)255; return p; };
  float* mod = (float*)alloc(6 * DM * 4);
  u16* h = (u16*)alloc((size_t)S * DM * 2);
  u16* Qx = (u16*)alloc((size_t)S * HD * 2); u16* Kx = (u16*)alloc((size_t)S * HD * 2); u16* Vx = (u16*)alloc((size_t)S * HD * 2);
  u16* qm = (u16*)alloc(MEM * HD * 2); u16* km = (u16*)alloc(MEM * HD * 2); u16* vm = (u16*)alloc(MEM * HD * 2);
  u16* o = (u16*)alloc(L * HD * 2); u16* oo = (u16*)alloc(L * DM * 2); u16* mem = (u16*)alloc(MEM * DM * 2);
  u16* x1 = (u16*)alloc((size_t)S * DM * 2); u16* h2 = (u16*)alloc((size_t)S * DM * 2); u16* f = (u16*)alloc((size_t)S * FF * 2);
  k_mod<<<15, 256, 0, stream>>>(c, wmod, mod);
  k_ln<<<S / 4, 256, 0, stream>>>(x, mod + DM, mod, h);
  k_gemm<0><<<dim3(HD / 64, S / 64), 256, 0, stream>>>(h, DM, Wq, HD, Qx, HD, S, HD, DM, nullptr, nullptr, nullptr);
  k_gemm<0><<<dim3(HD / 64, S / 64), 256, 0, stream>>>(h, DM, Wk, HD, Kx, HD, S, HD, DM, nullptr, nullptr, nullptr);
  k_gemm<0><<<dim3(HD / 64, S / 64), 256, 0, stream>>>(h, DM, Wv, HD, Vx, HD, S, HD, DM, nullptr, nullptr, nullptr);
  for (int s = 0; s < NSEG; ++s) {
    const u16* m = s == 0 ? mem0 : mem;
    k_gemm<0><<<dim3(HD / 64, 1), 256, 0, stream>>>(m, DM, Wq, HD, qm, HD, MEM, HD, DM, nullptr, nullptr, nullptr);
    k_gemm<0><<<dim3(HD / 64, 1), 256, 0, stream>>>(m, DM, Wk, HD, km, HD, MEM, HD, DM, nullptr, nullptr, nullptr);
    k_gemm<0><<<dim3(HD / 64, 1), 256, 0, stream>>>(m, DM, Wv, HD, vm, HD, MEM, HD, DM, nullptr, nullptr, nullptr);
    k_attn<<<H, 256, 0, stream>>>(qm, km, vm, Qx, Kx, Vx, s, o);
    k_gemm<0><<<dim3(DM / 64, 1), 256, 0, stream>>>(o, HD, Wo, DM, oo, DM, L, DM, HD, nullptr, nullptr, nullptr);
    k_scatter<<<(L * DM + 255) / 256, 256, 0, stream>>>(oo, mem, x, mod + 2 * DM, x1, s);
  }
  k_ln<<<S / 4, 256, 0, stream>>>(x1, mod + 4 * DM, mod + 3 * DM, h2);
  k_gemm<1><<<dim3(FF / 64, S / 64), 256, 0, stream>>>(h2, DM, w1, FF, f, FF, S, FF, DM, b1, nullptr, nullptr);
  k_gemm<2><<<dim3(DM / 64, S / 64), 256, 0, stream>>>(f, FF, w2, DM, (u16*)d_out, DM, S, DM, FF, b2, mod + 5 * DM, x1);
}
```

```cpp
#include <hip/hip_runtime.h>
#include <hip/hip_bf16.h>
#include <stdint.h>
#include <stdlib.h>

typedef unsigned short u16;
typedef __bf16 bf16x2 __attribute__((ext_vector_type(2)));
typedef __bf16 bf16x4 __attribute__((ext_vector_type(4)));
typedef __bf16 bf16x8 __attribute__((ext_vector_type(8)));
typedef __bf16 bf16x16 __attribute__((ext_vector_type(16)));
typedef float f32x2 __attribute__((ext_vector_type(2)));
typedef float f32x4 __attribute__((ext_vector_type(4)));
typedef unsigned v2u __attribute__((ext_vector_type(2)));
typedef unsigned v4u __attribute__((ext_vector_type(4)));
typedef unsigned v8u __attribute__((ext_vector_type(8)));
#define GAS __attribute__((address_space(1)))
#define LAS __attribute__((address_space(3)))
typedef GAS unsigned gu32;
#define RLX_AGENT __ATOMIC_RELAXED, __HIP_MEMORY_SCOPE_AGENT
typedef __amdgpu_buffer_rsrc_t rsrc_t;

constexpr int S = 2048, DM = 640, H = 32, DH = 192, HD = H * DH, NSEG = 64, FF = 1536;
constexpr float SCALE = 0.07216878364870322f, LOG2E = 1.4426950408889634f;

constexpr unsigned XSTEP = 131072;
constexpr unsigned XP = 10240, XT1 = 30720, XT2 = 31232;
__host__ __device__ constexpr unsigned xmap(unsigned o) { return ((o >> 13) << 14) + (o & 8191u); }
constexpr size_t O_XR = 0, O_CTL = O_XR + (size_t)NSEG * XSTEP, O_SEL = O_CTL + 65536, O_MOD = O_SEL + 4096, O_MODP = O_MOD + 16384, O_HX = O_MODP + 8192;
constexpr size_t SZ_ACT = (size_t)S * DM * 2;
constexpr size_t O_H2X = O_HX + SZ_ACT, O_X1 = O_H2X + SZ_ACT, O_KX = O_X1 + SZ_ACT, O_VX = O_KX + (size_t)S * HD * 2, O_UX = O_VX + (size_t)S * HD * 2;
constexpr size_t O_UM = O_UX + (size_t)S * H * DM * 2, O_KM = O_UM + (size_t)NSEG * 80 * 4096, O_PX = O_KM + (size_t)NSEG * H * 6144;
constexpr size_t O_PM = O_PX + (size_t)NSEG * 65536, O_FX = O_PM + (size_t)NSEG * 16384, O_BT = O_FX + (size_t)S * FF * 2, O_DBG = O_BT + (size_t)H * DM * DM * 2;
constexpr size_t WS_NEED = O_DBG + (4u << 20);
constexpr int C_TICK = 0, C_ABORT = 256, C_LNC = 288, C_KF = 512, C_VF = 1536, C_MF1 = 2560, C_UF = 3584, C_PF = 3712, C_MF2 = 3840, C_XC = 4224,
              C_HF2 = 6272, C_FF = 8320, C_PUT = 8416, C_HF1 = 8448, C_HG2 = 8704, C_PT = 8960, C_SCH = 9216, C_WORDS = 9248;

struct Args {
  const u16 *x, *mem0, *Wq, *Wk, *Wv, *Wo, *w1, *b1, *w2, *b2, *c, *wmod;
  char* ws; u16* out;
  int w1q, w2q;
};

__device__ __forceinline__ float bf2f(u16 v) { return __uint_as_float(((unsigned)v) << 16); }
__device__ __forceinline__ float blo(unsigned v) { return __uint_as_float(v << 16); }
__device__ __forceinline__ float bhi(unsigned v) { return __uint_as_float(v & 0xFFFF0000u); }
__device__ __forceinline__ unsigned cvtpk(float a, float b) { return __builtin_bit_cast(unsigned, __builtin_convertvector((f32x2){a, b}, bf16x2)); }
__device__ __forceinline__ float rbf(float f) { return blo(cvtpk(f, f)); }
template <typename T> __device__ __forceinline__ T ldsld(unsigned a) { return *(LAS T*)(size_t)a; }
template <typename T> __device__ __forceinline__ void ldsst(unsigned a, T v) { *(LAS T*)(size_t)a = v; }
#define SB() __builtin_amdgcn_sched_barrier(0)
__device__ __forceinline__ void barx() { __builtin_amdgcn_s_waitcnt(0xC07F); __builtin_amdgcn_s_barrier(); SB(); }
#define VMWAIT(n) asm volatile("s_waitcnt vmcnt(" #n ")" ::: "memory")
__device__ __forceinline__ rsrc_t mkrs(const void* p, size_t bytes) { return __builtin_amdgcn_make_buffer_rsrc((void*)p, 0, (unsigned)bytes, 0x00020000); }
__device__ __forceinline__ v4u ld16(rsrc_t r, unsigned vo, unsigned so) { return __builtin_amdgcn_raw_buffer_load_b128(r, vo, so, 16); }
__device__ __forceinline__ v2u ld8(rsrc_t r, unsigned vo, unsigned so) { return __builtin_amdgcn_raw_buffer_load_b64(r, vo, so, 16); }
__device__ __forceinline__ unsigned ld4(rsrc_t r, unsigned vo, unsigned so) { return __builtin_amdgcn_raw_buffer_load_b32(r, vo, so, 16); }
__device__ __forceinline__ void st16(v4u v, rsrc_t r, unsigned vo, unsigned so) {
  __builtin_amdgcn_raw_buffer_store_b128(v, r, vo, so, 16);
  asm volatile("s_nop 1" :: "v"(v));
}
__device__ __forceinline__ void st8(v2u v, rsrc_t r, unsigned vo, unsigned so) { __builtin_amdgcn_raw_buffer_store_b64(v, r, vo, so, 16); }
__device__ __forceinline__ void st4(unsigned v, rsrc_t r, unsigned vo, unsigned so) { __builtin_amdgcn_raw_buffer_store_b32(v, r, vo, so, 16); }
__device__ __forceinline__ f32x4 smf(v4u a, v8u b, f32x4 c, int idx) {
  return __builtin_amdgcn_smfmac_f32_16x16x64_bf16(__builtin_bit_cast(bf16x8, a), __builtin_bit_cast(bf16x16, b), c, idx, 0, 0);
}
__device__ __forceinline__ f32x4 mf32(v4u a, v4u b, f32x4 c) {
  return __builtin_amdgcn_mfma_f32_16x16x32_bf16(__builtin_bit_cast(bf16x8, a), __builtin_bit_cast(bf16x8, b), c, 0, 0, 0);
}
__device__ __forceinline__ v2u trrd(unsigned a) { return __builtin_bit_cast(v2u, __builtin_amdgcn_ds_read_tr16_b64_v4bf16((LAS bf16x4*)(size_t)a)); }
__device__ __forceinline__ v2u pack4(f32x4 v) { return (v2u){cvtpk(v[0], v[1]), cvtpk(v[2], v[3])}; }
__device__ __forceinline__ v4u cat(v2u a, v2u b) { return (v4u){a.x, a.y, b.x, b.y}; }
__device__ __forceinline__ v8u cat8(v4u a, v4u b) { return __builtin_shufflevector(a, b, 0, 1, 2, 3, 4, 5, 6, 7); }
__device__ __forceinline__ void dma16(const void* gsrc, unsigned lds_dst) {
  unsigned keep;
  asm volatile("s_mov_b32 %0, m0\n\ts_mov_b32 m0, %2\n\ts_nop 0\n\tglobal_load_lds_dwordx4 %1, off sc1\n\ts_mov_b32 m0, %0"
               : "=&s"(keep) : "v"(gsrc), "s"(lds_dst) : "memory");
}
__device__ __forceinline__ void dma16x4(const void* gsrc, unsigned lds_dst) {
  unsigned keep;
  asm volatile("s_mov_b32 %0, m0\n\ts_mov_b32 m0, %2\n\ts_nop 0\n\tglobal_load_lds_dwordx4 %1, off sc1\n\tglobal_load_lds_dwordx4 %1, off offset:1024 sc1\n\t"
               "global_load_lds_dwordx4 %1, off offset:2048 sc1\n\tglobal_load_lds_dwordx4 %1, off offset:3072 sc1\n\ts_mov_b32 m0, %0"
               : "=&s"(keep) : "v"(gsrc), "s"(lds_dst) : "memory");
}
__device__ __forceinline__ void dma16x2(const void* gsrc, unsigned lds_dst) {
  unsigned keep;
  asm volatile("s_mov_b32 %0, m0\n\ts_mov_b32 m0, %2\n\ts_nop 0\n\tglobal_load_lds_dwordx4 %1, off sc1\n\tglobal_load_lds_dwordx4 %1, off offset:1024 sc1\n\t"
               "s_mov_b32 m0, %0" : "=&s"(keep) : "v"(gsrc), "s"(lds_dst) : "memory");
}
__device__ __forceinline__ void dma16p(const void* gsrc, unsigned lds_dst) {
  unsigned keep;
  asm volatile("s_mov_b32 %0, m0\n\ts_mov_b32 m0, %2\n\ts_nop 0\n\tglobal_load_lds_dwordx4 %1, off\n\ts_mov_b32 m0, %0"
               : "=&s"(keep) : "v"(gsrc), "s"(lds_dst) : "memory");
}
__device__ __forceinline__ unsigned xcc_id() { return (unsigned)__builtin_amdgcn_s_getreg(20 | (3 << 11)) & 0xfu; }
__device__ __forceinline__ unsigned umax4(v4u v) { return max(max(v.x, v.y), max(v.z, v.w)); }
__device__ __forceinline__ unsigned rt32() { return (unsigned)__builtin_amdgcn_s_memrealtime(); }
__device__ __forceinline__ unsigned cy32() { return (unsigned)__builtin_amdgcn_s_memtime(); }
__device__ __forceinline__ void wait_cy(unsigned c) { while ((int)(cy32() - c) < 0) {} }
template <int CTRL> __device__ __forceinline__ float dppf(float v) {
  return __builtin_bit_cast(float, __builtin_amdgcn_update_dpp(0, __builtin_bit_cast(int, v), CTRL, 0xf, 0xf, true));
}
__device__ __forceinline__ float row16_sum(float x) { x += dppf<0xB1>(x); x += dppf<0x4E>(x); x += dppf<0x141>(x); x += dppf<0x140>(x); return x; }
__device__ __forceinline__ float xq_max(float x) {
  auto s = __builtin_amdgcn_permlane16_swap(__float_as_uint(x), __float_as_uint(x), false, false);
  x = fmaxf(__uint_as_float(s[0]), __uint_as_float(s[1]));
  auto t = __builtin_amdgcn_permlane32_swap(__float_as_uint(x), __float_as_uint(x), false, false);
  return fmaxf(__uint_as_float(t[0]), __uint_as_float(t[1]));
}
__device__ __forceinline__ float xq_sum(float x) {
  auto s = __builtin_amdgcn_permlane16_swap(__float_as_uint(x), __float_as_uint(x), false, false);
  x = __uint_as_float(s[0]) + __uint_as_float(s[1]);
  auto t = __builtin_amdgcn_permlane32_swap(__float_as_uint(x), __float_as_uint(x), false, false);
  return __uint_as_float(t[0]) + __uint_as_float(t[1]);
}
__host__ __device__ constexpr int smf_col(int qb, int j) {
  return 32 * (j >> 3) + 16 * (qb >> 1) + 8 * (((j & 7) >> 1) & 1) + 4 * (qb & 1) + 2 * ((j & 7) >> 2) + (j & 1);
}

#define STAMP(slot_, i_) do {} while (0)
struct Ctx { rsrc_t rc; gu32* abortw; bool dead; unsigned slot; int seg; };
__device__ __forceinline__ bool flags_ready(const Ctx& c, unsigned vo, unsigned need) {
  const unsigned v = ld4(c.rc, vo, 0);
  asm volatile("" ::: "memory");
  return !__any(v < need);
}
__device__ __forceinline__ void flags_wait(Ctx& c, unsigned vo, unsigned need) {
#pragma unroll 1
  for (unsigned spins = 0; !c.dead; ++spins) {
    if (flags_ready(c, vo, need)) return;
    __builtin_amdgcn_s_sleep(4);
    if ((spins & 63u) == 63u) {
      if (spins > 40000u) __hip_atomic_store(c.abortw, 1u, RLX_AGENT);
      if (__builtin_amdgcn_readfirstlane(__hip_atomic_load(c.abortw, RLX_AGENT))) c.dead = true;
    }
  }
}
template <int BASE>
__device__ __forceinline__ void seg_wait(Ctx& c, int s, int& known, int lane) {
#pragma unroll 1
  for (unsigned spins = 0; known <= s && !c.dead; ++spins) {
    const unsigned v = ld4(c.rc, (BASE + lane * 4) * 4, 0);
    asm volatile("" ::: "memory");
    const unsigned long long miss = __ballot(v == 0u);
    known = miss ? __builtin_ctzll(miss) : 64;
    if (known > s) return;
    __builtin_amdgcn_s_sleep(4);
    if ((spins & 63u) == 63u) {
      if (spins > 40000u) __hip_atomic_store(c.abortw, 1u, RLX_AGENT);
      if (__builtin_amdgcn_readfirstlane(__hip_atomic_load(c.abortw, RLX_AGENT))) c.dead = true;
    }
  }
}
__device__ __forceinline__ unsigned ld4_async(const void* p) {
  unsigned v;
  asm volatile("global_load_dword %0, %1, off sc1" : "=v"(v) : "v"(p) : "memory");
  return v;
}
#define PIN(x) asm volatile("" : "+v"(x))
__device__ __forceinline__ void flag_set(const Ctx& c, unsigned word, unsigned val) { st4(val, c.rc, word * 4u, 0); }

constexpr int PRE_BT = 256, PRE_MOD = 60, PRE_CAL = 16, PRE_GRID = PRE_BT + PRE_MOD + PRE_CAL;

__global__ void __launch_bounds__(256, 1) k_pre(Args a) {
  __shared__ __attribute__((aligned(16))) char sm[32768];
  const unsigned L0 = (unsigned)(size_t)(LAS char*)sm;
  const int tid = threadIdx.x, lane = tid & 63, w = __builtin_amdgcn_readfirstlane(tid >> 6), b = blockIdx.x;
  if (b < PRE_BT) {
    const int h = b >> 3, part = b & 7;
    for (int i = tid; i < 192 * 10; i += 256) {
      const int e = i / 10, ch = i % 10;
      ldsst<v4u>(L0 + e * 160 + ch * 16, *(const v4u*)(a.Wo + ((size_t)h * DH + e) * DM + part * 80 + ch * 8));
    }
    for (int i = b * 256 + tid; i < NSEG * 4096; i += PRE_BT * 256) {
      const unsigned s = (unsigned)i >> 12, byte = ((unsigned)i & 4095u) * 16;
      v4u v = {0xFFFFFFFFu, 0xFFFFFFFFu, 0xFFFFFFFFu, 0xFFFFFFFFu};
      const unsigned o = ((byte >> 14) << 13) + (byte & 8191u);
      if (s == 0 && o < 10240) {
        v = *(const v4u*)(a.mem0 + ((o >> 4) & 7) * DM + (o >> 7) * 8);
        for (int k = 0; k < 4; ++k) v[k] = min(v[k], 0xFFFFFFFEu);
      }
      *(v4u*)(a.ws + O_XR + (size_t)s * XSTEP + byte) = v;
    }
    if (b * 256 + tid < C_WORDS) ((unsigned*)(a.ws + O_CTL))[b * 256 + tid] = 0u;
    __syncthreads();
    v4u bw[6][5];
#pragma unroll
    for (int ks = 0; ks < 6; ++ks)
#pragma unroll
      for (int nt = 0; nt < 5; ++nt) {
        const unsigned ad = L0 + (32 * ks + 8 * (lane >> 4) + ((lane & 15) >> 2)) * 160 + nt * 32 + (lane & 3) * 8;
        bw[ks][nt] = cat(trrd(ad), trrd(ad + 4 * 160));
      }
    for (int T = w; T < 10; T += 4) {
      f32x4 acc[4][5];
#pragma unroll
      for (int tp = 0; tp < 4; ++tp) {
        const int k = 64 * T + smf_col((lane & 15) >> 2, 4 * tp + (lane & 3));
        const u16* ap = a.Wv + (size_t)k * HD + h * DH + 8 * (lane >> 4);
        v4u af[6];
#pragma unroll
        for (int ks = 0; ks < 6; ++ks) af[ks] = *(const v4u*)(ap + 32 * ks);
#pragma unroll
        for (int nt = 0; nt < 5; ++nt) {
          acc[tp][nt] = (f32x4){0.f, 0.f, 0.f, 0.f};
#pragma unroll
          for (int ks = 0; ks < 6; ++ks) acc[tp][nt] = mf32(af[ks], bw[ks][nt], acc[tp][nt]);
        }
      }
#pragma unroll
      for (int nt = 0; nt < 5; ++nt) {
        const int cg = part * 10 + 2 * nt + ((lane & 15) >> 3);
        char* dst = a.ws + O_BT + ((size_t)((cg * 4 + (h >> 3)) * 40 + T * 4 + ((h >> 1) & 3))) * 2048 + ((lane >> 4) * 16 + (h & 1) * 8 + (lane & 7)) * 32;
        *(v4u*)dst = cat(pack4(acc[0][nt]), pack4(acc[1][nt]));
        *(v4u*)(dst + 16) = cat(pack4(acc[2][nt]), pack4(acc[3][nt]));
      }
    }
  } else if (b < PRE_BT + PRE_MOD) {
    const int n0 = (b - PRE_BT) * 64 + (tid & 7) * 8, k0 = (tid >> 3) * 20;
    float acc[8] = {0.f, 0.f, 0.f, 0.f, 0.f, 0.f, 0.f, 0.f};
    v4u wv[20];
#pragma unroll
    for (int k = 0; k < 20; ++k) wv[k] = *(const v4u*)(a.wmod + (size_t)(k0 + k) * (6 * DM) + n0);
#pragma unroll
    for (int k = 0; k < 20; ++k) {
      const float cv = bf2f(a.c[k0 + k]);
      const float sc = rbf(cv / (1.f + __expf(-cv)));
#pragma unroll
      for (int j = 0; j < 4; ++j) { acc[2 * j] += sc * blo(wv[k][j]); acc[2 * j + 1] += sc * bhi(wv[k][j]); }
    }
#pragma unroll
    for (int j = 0; j < 8; ++j) ldsst<float>(L0 + ((tid >> 3) * 64 + (tid & 7) * 8 + j) * 4, acc[j]);
    __syncthreads();
    if (tid < 64) {
      float s = 0.f;
      for (int k = 0; k < 32; ++k) s += ldsld<float>(L0 + (k * 64 + tid) * 4);
      const int n = (b - PRE_BT) * 64 + tid, which = n / DM;
      const float v = rbf(s);
      ((float*)(a.ws + O_MOD))[n] = v;
      if (which != 2 && which != 5) {
        const bool isc = which == 1 || which == 4;
        ((u16*)(a.ws + O_MODP))[((which >= 3 ? 2 : 0) + (isc ? 0 : 1)) * DM + n % DM] = (u16)(cvtpk(isc ? 1.f + v : v, 0.f) & 0xFFFFu);
      }
    }
  } else {
    const int s = (b - PRE_BT - PRE_MOD) * 4 + w;
    const rsrc_t r = mkrs(a.ws + O_XR, (size_t)NSEG * XSTEP);
    unsigned best[2] = {0xFFFFFFFFu, 0xFFFFFFFFu};
    for (int p = 0; p < 4; ++p)
      for (int hf = 0; hf < 2; ++hf) {
        const unsigned off = s * XSTEP + 65536u + hf * 8192u + p * 2048u;
        st4(0u, r, off, 0);
        VMWAIT(0);
        const unsigned t0 = cy32();
        asm volatile("s_waitcnt lgkmcnt(0)" ::: "memory");
        const unsigned v = ld4(r, off, 0);
        asm volatile("s_waitcnt vmcnt(0)" :: "v"(v) : "memory");
        best[hf] = min(best[hf], cy32() - t0);
      }
    unsigned nearh = best[1] < best[0] ? 1u : 0u;
    if (xcc_id() >= 4) nearh ^= 1u;
    if (lane == 0) ((unsigned*)(a.ws + O_SEL))[s] = nearh;
  }
}

template <bool COH>
__device__ __forceinline__ void ln_segment(const char* src, const char* gs, rsrc_t rdst, unsigned dbase, int w, int lane) {
  const rsrc_t rs = mkrs(src, 32 * DM * 2);
  const int c16 = lane & 15, row0 = w * 8 + (lane >> 4);
  v4u v[2][5], g[5], sh[5];
#pragma unroll
  for (int p = 0; p < 2; ++p)
#pragma unroll
    for (int i = 0; i < 5; ++i) v[p][i] = __builtin_amdgcn_raw_buffer_load_b128(rs, (row0 + 4 * p) * (DM * 2) + (c16 + 16 * i) * 16, 0, COH ? 16 : 0);
#pragma unroll
  for (int i = 0; i < 5; ++i) { g[i] = *(const v4u*)(gs + (c16 + 16 * i) * 16); sh[i] = *(const v4u*)(gs + DM * 2 + (c16 + 16 * i) * 16); }
#pragma unroll
  for (int p = 0; p < 2; ++p) {
    f32x2 f[20];
    f32x2 s2 = {0.f, 0.f};
#pragma unroll
    for (int i = 0; i < 20; ++i) { f[i] = (f32x2){blo(v[p][i >> 2][i & 3]), bhi(v[p][i >> 2][i & 3])}; s2 += f[i]; }
    const float mean = row16_sum(s2.x + s2.y) * (1.f / DM);
    f32x2 q2 = {0.f, 0.f};
#pragma unroll
    for (int i = 0; i < 20; ++i) { const f32x2 d = f[i] - mean; q2 += d * d; }
    const float m = rbf(mean), var = rbf(row16_sum(q2.x + q2.y) * (1.f / DM));
    const float inv = 1.f / rbf(sqrtf(rbf(var + 1e-5f)));
    const int row = row0 + 4 * p;
#pragma unroll
    for (int i = 0; i < 5; ++i) {
      v4u o;
#pragma unroll
      for (int j = 0; j < 4; ++j) {
        const f32x2 dd = f[4 * i + j] - m;
        const unsigned d = cvtpk(dd.x, dd.y);
        const f32x2 nn = (f32x2){blo(d), bhi(d)} * inv;
        const unsigned n = cvtpk(nn.x, nn.y);
        const f32x2 tt = (f32x2){blo(n), bhi(n)} * (f32x2){blo(g[i][j]), bhi(g[i][j])};
        const unsigned t = cvtpk(tt.x, tt.y);
        const f32x2 oo = (f32x2){blo(t), bhi(t)} + (f32x2){blo(sh[i][j]), bhi(sh[i][j])};
        o[j] = cvtpk(oo.x, oo.y);
      }
      const int ch = c16 + 16 * i;
      st16(o, rdst, ((row >> 4) * 20 + (ch >> 2)) * 1024 + ((ch & 3) * 16 + (row & 15)) * 16, dbase);
    }
  }
}

struct Sched {
  unsigned g1, g2, e1, e2; int r1, r2, w1, w2, c1, c2;
  __device__ __forceinline__ void init(int W1, int W2) { g1 = g2 = e1 = e2 = 0; r1 = r2 = -1; w1 = W1; w2 = W2; c1 = c2 = 0; }
  static __device__ __forceinline__ int upd(int r, int x, int c) {
    x = min(max(x, 4), 1200);
    const int f = x > r ? min(x, r + 24) : (r + x) >> 1;
    return c == 0 ? r : (r < 0 ? x : (c == 2 ? f : r));
  }
  __device__ __forceinline__ void got_a(unsigned a) {
    const bool ok = r2 >= 0 && (int)(g1 - a) >= w1 - 12;
    e1 = ok ? g1 : a + w1 + 48; c1 = ok ? 2 : 1;
    r2 = upd(r2, (int)(a - e2), c2);
    g2 = e1 + r1 + w2;
  }
  __device__ __forceinline__ void got_b(unsigned b) {
    const bool ok = r1 >= 0 && (int)(g2 - b) >= w2 - 12;
    e2 = ok ? g2 : b + w2 + 48; c2 = ok ? 2 : 1;
    r1 = upd(r1, (int)(b - e1), c1);
    g1 = e2 + r2 + w1;
  }
};
struct Clock {
  unsigned rt0, cy0, rte, cye; float upc, cpu;
  static __device__ __forceinline__ void edge(unsigned& r, unsigned& c) {
    const unsigned r0 = rt32();
#pragma unroll 1
    for (int k = 0; k < 64; ++k) { r = rt32(); c = cy32(); if (r != r0) break; }
  }
  __device__ __forceinline__ void init() { edge(rt0, cy0); rte = rt0; cye = cy0; cpu = 5.9f; upc = 1.f / 5.9f; }
  __device__ __forceinline__ void sync() {
    edge(rte, cye);
    const float dr = (float)(rte - rt0) * 4.f, dc = (float)(cye - cy0);
    cpu = __builtin_bit_cast(float, __builtin_amdgcn_readfirstlane(__builtin_bit_cast(int, dc * __builtin_amdgcn_rcpf(dr))));
    upc = __builtin_bit_cast(float, __builtin_amdgcn_readfirstlane(__builtin_bit_cast(int, dr * __builtin_amdgcn_rcpf(dc))));
  }
  __device__ __forceinline__ unsigned time_of(unsigned c) const { return rte * 4u + (unsigned)__builtin_amdgcn_readfirstlane((int)((float)(int)(c - cye) * upc)); }
  __device__ __forceinline__ unsigned cycle_of(unsigned t) const { return cye + (unsigned)__builtin_amdgcn_readfirstlane((int)((float)(int)(t - rte * 4u) * cpu)); }
};
#define DPPM " row_mask:0xf bank_mask:0xf\n\t"
__device__ __forceinline__ int wave_max_i32(int v) {
  int x, y;
  asm("s_nop 1\n\t"
      "v_max_i32_dpp %0, %2, %2 quad_perm:[1,0,3,2]" DPPM "s_nop 1\n\t"
      "v_max_i32_dpp %0, %0, %0 quad_perm:[2,3,0,1]" DPPM "s_nop 1\n\t"
      "v_max_i32_dpp %0, %0, %0 row_half_mirror" DPPM "s_nop 1\n\t"
      "v_max_i32_dpp %1, %0, %0 row_mirror" DPPM
      "v_max_i32_dpp %0, %0, %0 row_mirror" DPPM "s_nop 1\n\t"
      "v_permlane16_swap_b32 %0, %1\n\ts_nop 0\n\t"
      "v_max_i32 %0, %0, %1\n\t"
      "v_mov_b32 %1, %0\n\ts_nop 1\n\t"
      "v_permlane32_swap_b32 %0, %1\n\ts_nop 0\n\t"
      "v_max_i32 %0, %0, %1\n\ts_nop 0" : "=&v"(x), "=&v"(y) : "v"(v));
  return __builtin_amdgcn_readfirstlane(x);
}
__device__ __forceinline__ float red32_max(float a, float b) {
  float x, y;
  asm("v_max_f32 %0, %2, %3\n\ts_nop 1\n\t"
      "v_max_f32_dpp %0, %0, %0 quad_perm:[1,0,3,2]" DPPM "s_nop 1\n\t"
      "v_max_f32_dpp %0, %0, %0 quad_perm:[2,3,0,1]" DPPM "s_nop 1\n\t"
      "v_max_f32_dpp %0, %0, %0 row_half_mirror" DPPM "s_nop 1\n\t"
      "v_max_f32_dpp %1, %0, %0 row_mirror" DPPM
      "v_max_f32_dpp %0, %0, %0 row_mirror" DPPM "s_nop 1\n\t"
      "v_permlane16_swap_b32 %0, %1\n\ts_nop 0\n\t"
      "v_max_f32 %0, %0, %1" : "=&v"(x), "=&v"(y) : "v"(a), "v"(b));
  return x;
}
__device__ __forceinline__ float red32_sum(float a, float b) {
  float x, y;
  asm("v_add_f32 %0, %2, %3\n\ts_nop 1\n\t"
      "v_add_f32_dpp %0, %0, %0 quad_perm:[1,0,3,2]" DPPM "s_nop 1\n\t"
      "v_add_f32_dpp %0, %0, %0 quad_perm:[2,3,0,1]" DPPM "s_nop 1\n\t"
      "v_add_f32_dpp %0, %0, %0 row_half_mirror" DPPM "s_nop 1\n\t"
      "v_add_f32_dpp %1, %0, %0 row_mirror" DPPM
      "v_add_f32_dpp %0, %0, %0 row_mirror" DPPM "s_nop 1\n\t"
      "v_permlane16_swap_b32 %0, %1\n\ts_nop 0\n\t"
      "v_add_f32 %0, %0, %1" : "=&v"(x), "=&v"(y) : "v"(a), "v"(b));
  return x;
}
struct Stamps { unsigned t0, t1; };
__device__ __forceinline__ Stamps stamps_load(rsrc_t rx, unsigned o0, unsigned o1, unsigned sbase) { return Stamps{ld4(rx, o0, sbase), ld4(rx, o1, sbase)}; }
__device__ __forceinline__ unsigned stamps_latest(Stamps s, rsrc_t rx, unsigned o0, unsigned o1, unsigned sbase, unsigned ref, bool dead) {
#pragma unroll 1
  for (int k = 0; k < 4000; ++k) {
    if (!__any(s.t0 == 0xFFFFFFFFu || s.t1 == 0xFFFFFFFFu) || dead) break;
    __builtin_amdgcn_s_sleep(1);
    asm volatile("" ::: "memory");
    s = stamps_load(rx, o0, o1, sbase);
  }
  return ref + (unsigned)wave_max_i32(max((int)(s.t0 - ref), (int)(s.t1 - ref)));
}
template <int NB>
__device__ __forceinline__ void poll(rsrc_t r, const unsigned (&voff)[NB], unsigned soff, v4u (&v)[NB], Ctx& c) {
#pragma unroll 1
  for (unsigned spins = 0;; ++spins) {
#pragma unroll
    for (int k = 0; k < NB; ++k) v[k] = ld16(r, voff[k], soff);
    unsigned m = 0;
#pragma unroll
    for (int k = 0; k < NB; ++k) m = max(m, umax4(v[k]));
    asm volatile("" ::: "memory");
    if (!__any(m == 0xFFFFFFFFu) || c.dead) return;
    if ((spins & 255u) == 255u) {
      if (spins > 60000u) __hip_atomic_store(c.abortw, 1u, RLX_AGENT);
      if (__builtin_amdgcn_readfirstlane(__hip_atomic_load(c.abortw, RLX_AGENT))) c.dead = true;
    }
  }
}
__device__ __forceinline__ v2u fold_pack(f32x4 a, f32x4 b) {
  float f[4];
#pragma unroll
  for (int r = 0; r < 4; ++r) {
    auto sw = __builtin_amdgcn_permlane32_swap(__float_as_uint(a[r]), __float_as_uint(b[r]), false, false);
    f[r] = __uint_as_float(sw[0]) + __uint_as_float(sw[1]);
  }
  return (v2u){cvtpk(f[0], f[1]), cvtpk(f[2], f[3])};
}

__device__ __forceinline__ void stage384(const char* W, size_t ldb, int row0, int n3, unsigned dst, int w, int lane) {
#pragma unroll 1
  for (int i = w; i < 3 * n3; i += 4) {
    const unsigned byte = (i % 3) * 1024 + lane * 16, row = byte / 384, col = byte % 384;
    dma16p(W + (size_t)(row0 + 8 * (i / 3) + row) * ldb + col, dst + i * 1024);
  }
}

constexpr int L1_IMG = 0, L1_PART = 10240, L1_W = L1_PART + 6144, MAIN_LDS = L1_W + 4 * 18 * 2048;
static_assert(MAIN_LDS == 163840, "lds");

__device__ __forceinline__ void layer1(const Args& a, const int h, const unsigned L0, Ctx& cx) {
  const int tid = threadIdx.x, lane = tid & 63, w = __builtin_amdgcn_readfirstlane(tid >> 6);
  const rsrc_t rx = mkrs(a.ws + O_XR, (size_t)NSEG * XSTEP), rk = mkrs(a.ws + O_KX, (size_t)S * HD * 2), rkm = mkrs(a.ws + O_KM, (size_t)NSEG * H * 6144);
  const unsigned wl = L0 + L1_W + w * 18 * 2048 + lane * 16;
  v8u wq[3][10], wk[3][4];
  {
    const unsigned trb = (16 * (lane >> 5) + 4 * ((lane >> 4) & 1) + ((lane & 15) >> 2)) * 384 + (3 * w) * 32 + (lane & 3) * 8;
    auto frag = [&](unsigned base, int t) {
      const unsigned ad = base + trb + t * 32;
      const v2u r0 = trrd(ad), r1 = trrd(ad + 8 * 384), r2 = trrd(ad + 32 * 384), r3 = trrd(ad + 40 * 384);
      return (v8u){r0.x, r1.x, r0.y, r1.y, r2.x, r3.x, r2.y, r3.y};
    };
    auto stage = [&](int r) {
      const u16* W = (r < 5 ? a.Wq : a.Wk) + h * DH;
      stage384((const char*)W, HD * 2, 128 * (r % 5), 16, L0 + (r & 1) * 49152, w, lane);
    };
    v8u tmp[6][3];
    stage(0); stage(1);
#pragma unroll
    for (int r = 0; r < 10; ++r) {
      if (r < 9) VMWAIT(12); else VMWAIT(0);
      barx();
#pragma unroll
      for (int k = 0; k < 2; ++k)
#pragma unroll
        for (int t = 0; t < 3; ++t) {
          const int T = 2 * (r % 5) + k;
          const v8u f = frag(L0 + (r & 1) * 49152 + k * 24576, t);
          if (r < 5) wq[t][T] = f; else if (T < 4) wk[t][T < 4 ? T : 0] = f; else tmp[T < 4 ? 0 : T - 4][t] = f;
        }
      barx();
      if (r < 8) stage(r + 2);
    }
#pragma unroll
    for (int T = 4; T < 10; ++T)
#pragma unroll
      for (int t = 0; t < 3; ++t) {
        const v8u f = tmp[T - 4][t];
        ldsst<v4u>(wl + (t * 6 + T - 4) * 2048, __builtin_shufflevector(f, f, 0, 1, 2, 3));
        ldsst<v4u>(wl + (t * 6 + T - 4) * 2048 + 1024, __builtin_shufflevector(f, f, 4, 5, 6, 7));
      }
  }
  const unsigned long long selmask = __ballot(((const unsigned*)(a.ws + O_SEL))[lane] != 0u);
  const unsigned sel_lo = __builtin_amdgcn_readfirstlane((unsigned)selmask), sel_hi = __builtin_amdgcn_readfirstlane((unsigned)(selmask >> 32));
  const int sidx = (lane & 8) ? 0xEEEE : 0x4444;
  unsigned moff[3];
#pragma unroll
  for (int k = 0; k < 3; ++k) { int b = w + 4 * k; if (b >= 10) b = w; moff[k] = xmap(b * 1024 + lane * 16); }
  const unsigned mst2 = L0 + L1_IMG + (w < 2 ? w + 8 : w) * 1024 + lane * 16;
  const int qi = 2 * w + (lane >> 5), j = lane & 31;
  const unsigned poff = xmap(XP + (w >> 1) * 10240 + (w & 1) * 128 + (lane >> 5) * 16 +
                             ((j & 1) ? (8 + (h >> 4)) * 1024 + ((h >> 2) & 3) * 256 + (h & 3) * 32 + (j - 1) * 2
                                      : (h >> 2) * 1024 + (h & 3) * 256 + ((j & 15) >> 2) * 32 + ((j >> 4) * 4 + (j & 3)) * 2));
  const bool pstore = !(j & 1) || j < 8;
  const unsigned t1o = xmap(XT1 + (lane & 31) * 16), t2o0 = xmap(XT2 + lane * 16), t2o1 = xmap(XT2 + 1024 + (lane & 15) * 16);
  const int dq = (lane & 15) - 4 * (lane >> 4);
  const v4u ident = {dq == 0 ? 0x3F80u : dq == 1 ? 0x3F800000u : 0u, dq == 2 ? 0x3F80u : dq == 3 ? 0x3F800000u : 0u, 0u, 0u};
  const unsigned kxo = (h * 4 + w) * 3072 + lane * 16, kfo = (C_KF + h * 32) * 4;
  unsigned kready = 0;
  STAMP(cx.slot, 1);
  v4u kx[3];
  flags_wait(cx, kfo, 1u);
#pragma unroll
  for (int t = 0; t < 3; ++t) kx[t] = ld16(rk, kxo + t * 1024, 0);
  Sched sc; sc.init(a.w1q, a.w2q);
  Clock ck; ck.init();
  unsigned gate = cy32();
  const f32x4 Z4 = {0.f, 0.f, 0.f, 0.f};
#pragma unroll 1
  for (int s = 0; s < NSEG; ++s) {
    const unsigned sbase = s * XSTEP + (((s < 32 ? sel_lo : sel_hi) >> (s & 31)) & 1u) * 8192u;
    f32x4 aq[3], ak[3];
#pragma unroll
    for (int t = 0; t < 3; ++t) { aq[t] = Z4; ak[t] = Z4; asm volatile("" : "+v"(aq[t]), "+v"(ak[t])); }
    v4u llo[3], lhi[3];
#pragma unroll
    for (int i = 0; i < 3; ++i) { llo[i] = ldsld<v4u>(wl + i * 2048); lhi[i] = ldsld<v4u>(wl + i * 2048 + 1024); asm volatile("" : "+v"(llo[i]), "+v"(lhi[i])); }
    asm volatile("" : "+v"(kx[0]), "+v"(kx[1]), "+v"(kx[2]));
    SB();
    wait_cy(gate);
    v4u mv[3];
    poll<3>(rx, moff, sbase, mv, cx);
    ldsst<v4u>(L0 + L1_IMG + w * 1024 + lane * 16, mv[0]);
    ldsst<v4u>(L0 + L1_IMG + (w + 4) * 1024 + lane * 16, mv[1]);
    ldsst<v4u>(mst2, mv[2]);
    barx();
    v4u am[10];
#pragma unroll
    for (int T = 0; T < 10; ++T) am[T] = ldsld<v4u>(L0 + L1_IMG + T * 1024 + lane * 16);
    Stamps st2;
    unsigned kfv;
    SB();
    f32x4 s0 = Z4, s1 = Z4, sm = Z4, xt;
    v4u X01;
    v2u xa;
    unsigned sw[8];
    float ff[4];
#pragma unroll
    for (int t = 0; t < 3; ++t) {
#pragma unroll
      for (int T = 0; T < 10; ++T) {
        const int li = t * 6 + (T - 4);
        aq[t] = smf(am[T], wq[t][T], aq[t], sidx);
        SB();
        if (T >= 5 && li + 2 < 18) lhi[(li - 1) % 3] = ldsld<v4u>(wl + (li + 2) * 2048 + 1024);
        if (T == 0 && t > 0 && t * 6 + 2 < 18) lhi[(t * 6 - 1) % 3] = ldsld<v4u>(wl + (t * 6 + 2) * 2048 + 1024);
        if (t == 0 && T == 1) kfv = ld4(cx.rc, kfo, 0);
        if (t == 0 && T == 2) st2.t0 = ld4(rx, t2o0, sbase);
        if (t == 0 && T == 3) st2.t1 = ld4(rx, t2o1, sbase);
        if (t > 0) {
          if (T == 1) { for (int r = 0; r < 2; ++r) { auto x = __builtin_amdgcn_permlane32_swap(__float_as_uint(aq[t - 1][r]), __float_as_uint(ak[t - 1][r]), false, false); sw[2 * r] = x[0]; sw[2 * r + 1] = x[1]; } }
          if (T == 2) { ff[0] = __uint_as_float(sw[0]) + __uint_as_float(sw[1]); ff[1] = __uint_as_float(sw[2]) + __uint_as_float(sw[3]); }
          if (T == 3) { xa.x = cvtpk(ff[0], ff[1]); xa.y = cvtpk(ff[2], ff[3]); }
          if (T == 5) X01[2 * (t - 1)] = cvtpk(xt[0], xt[1]);
          if (t == 2 && T == 7) s0 = mf32(kx[0], X01, s0);
          if (t == 2 && T == 8) s1 = mf32(kx[1], X01, s1);
          if (t == 2 && T == 9) sm = mf32(X01, X01, sm);
        }
        SB();
        ak[t] = smf(am[T], T < 4 ? wk[t][T < 4 ? T : 0] : cat8(llo[(li + 18) % 3], lhi[(li + 18) % 3]), ak[t], sidx);
        SB();
        if (T >= 4 && li + 3 < 18) llo[li % 3] = ldsld<v4u>(wl + (li + 3) * 2048);
        if (t > 0) {
          if (T == 1) { for (int r = 2; r < 4; ++r) { auto x = __builtin_amdgcn_permlane32_swap(__float_as_uint(aq[t - 1][r]), __float_as_uint(ak[t - 1][r]), false, false); sw[2 * r] = x[0]; sw[2 * r + 1] = x[1]; } }
          if (T == 2) { ff[2] = __uint_as_float(sw[4]) + __uint_as_float(sw[5]); ff[3] = __uint_as_float(sw[6]) + __uint_as_float(sw[7]); }
          if (T == 3) xt = mf32((v4u){xa.x, xa.y, 0u, 0u}, ident, Z4);
          if (T == 5) X01[2 * (t - 1) + 1] = cvtpk(xt[2], xt[3]);
        }
        SB();
      }
    }
    xa = fold_pack(aq[2], ak[2]);
    xt = mf32((v4u){xa.x, xa.y, 0u, 0u}, ident, Z4);
    const v2u X2 = pack4(xt);
    unsigned cy_pub = 0, t_pub = 0;
    if (s < NSEG - 1) {
      const v4u xz = {X2.x, X2.y, 0u, 0u};
      s0 = mf32(kx[2], xz, s0);
      s1 = mf32(kx[2], (v4u){0u, 0u, X2.x, X2.y}, s1);
      sm = mf32(xz, xz, sm);
      if ((lane & 8) == 0) {
        const unsigned pa = L0 + L1_PART + w * 1536 + (lane & 7) * 64 + (lane >> 4) * 16;
        ldsst<f32x4>(pa, s0); ldsst<f32x4>(pa + 512, s1); ldsst<f32x4>(pa + 1024, sm);
      }
      barx();
      const unsigned ra = L0 + L1_PART + (j >> 4) * 512 + qi * 64 + (j & 15) * 4;
      const unsigned rb = L0 + L1_PART + 1024 + qi * 64 + (8 + (j & 7)) * 4;
      const float x0 = ldsld<float>(ra), x1 = ldsld<float>(ra + 1536), x2 = ldsld<float>(ra + 3072), x3 = ldsld<float>(ra + 4608);
      const float y0 = ldsld<float>(rb), y1 = ldsld<float>(rb + 1536), y2 = ldsld<float>(rb + 3072), y3 = ldsld<float>(rb + 4608);
      SB();
      const unsigned r1 = cvtpk((x0 + x1) + (x2 + x3), (y0 + y1) + (y2 + y3));
      const unsigned r2 = cvtpk(blo(r1) * SCALE, bhi(r1) * SCALE);
      const float sx = blo(r2), sy = bhi(r2);
      const float mx = red32_max(sx, sy);
      const float ex = __builtin_amdgcn_exp2f((sx - mx) * LOG2E), ey = __builtin_amdgcn_exp2f((sy - mx) * LOG2E);
      const float inv = __builtin_amdgcn_rcpf(red32_sum(ex, j < 8 ? ey : 0.f));
      const float own = ((j & 1) ? ey : ex) * inv, snd = ((j & 1) ? ex : ey) * inv;
      const float oth = dppf<0xB1>(snd);
      const unsigned pv = min((j & 1) ? cvtpk(oth, own) : cvtpk(own, oth), 0xFFFFFFFEu);
      if (pstore) st4(pv, rx, poff, sbase);
      SB();
      cy_pub = cy32();
      t_pub = ck.time_of(cy_pub);
      if (tid == 0) st4(min(t_pub, 0xFFFFFFFEu), rx, xmap(XT1 + h * 16), sbase);
    }
    st16(X01, rkm, (h * 4 + w) * 1536 + lane * 16, s * (H * 6144));
    st8(X2, rkm, (h * 4 + w) * 1536 + 1024 + lane * 8, s * (H * 6144));
    if (s < NSEG - 2) {
      kready = __builtin_amdgcn_readfirstlane(kfv);
      if (kready < (unsigned)s + 2u) flags_wait(cx, kfo, s + 2);
#pragma unroll
      for (int t = 0; t < 3; ++t) kx[t] = ld16(rk, kxo + t * 1024, (s + 1) * (H * 12288));
    }
    if (s < NSEG - 1) {
      if (s > 0) sc.got_a(stamps_latest(st2, rx, t2o0, t2o1, sbase, t_pub, cx.dead));
      ck.sync();
      wait_cy(cy_pub + 440);
      sc.got_b(stamps_latest(stamps_load(rx, t1o, t1o, sbase), rx, t1o, t1o, sbase, t_pub, cx.dead));
      gate = sc.r2 >= 0 ? ck.cycle_of(sc.g1) : cy_pub;
    } else {
      VMWAIT(0);
    }
    if (lane == 0) __hip_atomic_fetch_add((gu32*)(a.ws + O_CTL) + C_MF1 + h * 32, 1u, RLX_AGENT);
  }
}

constexpr int L2_IMG = 0, L2_UM = 10240, L2_UX = L2_UM + 4096, L2_W = L2_UX + 2 * 16384, L2_MISC = L2_W + 4 * 8 * 2048;

__device__ __forceinline__ void layer2(const Args& a, const int cg, const unsigned L0, Ctx& cx) {
  const int tid = threadIdx.x, lane = tid & 63, w = __builtin_amdgcn_readfirstlane(tid >> 6);
  const rsrc_t rx = mkrs(a.ws + O_XR, (size_t)NSEG * XSTEP), rum = mkrs(a.ws + O_UM, (size_t)NSEG * 80 * 4096);
  const unsigned wl = L0 + L2_W + w * 8 * 2048 + lane * 16;
  v8u wr[8][4];
  {
    const v4u* src = (const v4u*)(a.ws + O_BT + ((size_t)(cg * 4 + w) * 40) * 2048) + lane * 2;
#pragma unroll
    for (int f = 0; f < 40; ++f) {
      const v4u lo = src[f * 128], hi = src[f * 128 + 1];
      if (f >= 32) { ldsst<v4u>(wl + (f - 32) * 2048, lo); ldsst<v4u>(wl + (f - 32) * 2048 + 1024, hi); }
      else wr[f >> 2][f & 3] = cat8(lo, hi);
    }
  }
  const unsigned long long selmask = __ballot(((const unsigned*)(a.ws + O_SEL))[lane] != 0u);
  const unsigned sel_lo = __builtin_amdgcn_readfirstlane((unsigned)selmask), sel_hi = __builtin_amdgcn_readfirstlane((unsigned)(selmask >> 32));
  const int sidx = (lane & 8) ? 0xEEEE : 0x4444;
  unsigned moff[4], po[10];
#pragma unroll
  for (int k = 0; k < 4; ++k) { int b = w + 3 * k; if (b >= 10) b = w; moff[k] = xmap(b * 1024 + lane * 16); }
#pragma unroll
  for (int u = 0; u < 10; ++u) po[u] = xmap(XP + (w & 1) * 10240 + u * 1024 + lane * 16);
  const unsigned mst0 = L0 + L2_IMG + w * 1024 + lane * 16, mst3 = L0 + L2_IMG + (w == 0 ? 9 : w) * 1024 + lane * 16;
  const unsigned umo = L0 + L2_UM + ((w >> 1) * 2 + (lane & 1)) * 1024 + ((2 * (w & 1)) * 16 + 4 * ((lane & 7) >> 1) + 2 * (lane >> 5) + ((lane >> 3) & 1)) * 16 + ((lane >> 4) & 1) * 8;
  const unsigned ooff = xmap(cg * 128 + (4 * (w & 1) + 2 * ((lane >> 3) & 1) + (lane & 1)) * 16 + (lane >> 4) * 4);
  const unsigned t1o = xmap(XT1 + (lane & 31) * 16), t2o0 = xmap(XT2 + lane * 16), t2o1 = xmap(XT2 + 1024 + (lane & 15) * 16);
  const unsigned ufo = (C_UF + (lane & 31) * 4) * 4;
  auto ux_dma = [&](int s) {
    const char* up = a.ws + O_UX + ((size_t)s * 80 + cg) * 16384 + lane * 16;
#pragma unroll
    for (int v = 0; v < 16; ++v) dma16(up + v * 1024, L0 + L2_UX + (s & 1) * 16384 + v * 1024);
  };
  STAMP(cx.slot, 1);
  if (w == 3) { flags_wait(cx, ufo, 1u); ux_dma(0); }
  unsigned ufv = 0;
  unsigned tref = cy32();
  Sched sc; sc.init(a.w1q, a.w2q);
  Clock ck; ck.init();
  unsigned pbase = 0;
  const f32x4 Z4 = {0.f, 0.f, 0.f, 0.f};
#pragma unroll 1
  for (int s = 0; s < NSEG; ++s) {
    const unsigned sbase = s * XSTEP + (((s < 32 ? sel_lo : sel_hi) >> (s & 31)) & 1u) * 8192u;
    const unsigned nbase = (s + 1) * XSTEP + (((s + 1 < 32 ? sel_lo : sel_hi) >> ((s + 1) & 31)) & 1u) * 8192u;
    f32x4 acc[4];
#pragma unroll
    for (int t = 0; t < 4; ++t) { acc[t] = Z4; asm volatile("" : "+v"(acc[t])); }
    Stamps st1 = {0u, 0u}, st2 = {0u, 0u};
    if (w == 1 && s > 0) st1 = stamps_load(rx, t1o, t1o, pbase);
    if (w < 2 && s > 0) ck.sync();
    if (w < 3) {
      if (s > 0) wait_cy(tref + a.w1q * 6);
      v4u mv[4];
      poll<4>(rx, moff, sbase, mv, cx);
      ldsst<v4u>(mst0, mv[0]); ldsst<v4u>(mst0 + 3072, mv[1]); ldsst<v4u>(mst0 + 6144, mv[2]); ldsst<v4u>(mst3, mv[3]);
    }
    barx();
    v4u am[10];
#pragma unroll
    for (int T = 0; T < 10; ++T) am[T] = ldsld<v4u>(L0 + L2_IMG + T * 1024 + lane * 16);
    if (w == 1 && s > 0) st2 = stamps_load(rx, t2o0, t2o1, sbase);
    bool early = false;
    if (w == 3) {
      VMWAIT(0);
      if (s > 0 && lane == 0) flag_set(cx, C_MF2 + cg * 4, s);
      early = s + 1 < NSEG - 1 && !__any(ufv < (unsigned)s + 2u);
      if (early) ux_dma(s + 1);
    }
    SB();
    v4u lw[16];
#pragma unroll
    for (int T = 0; T < 10; ++T)
#pragma unroll
      for (int t = 0; t < 4; ++t) {
        const int i = T * 4 + t;
        acc[t] = smf(am[T], T < 8 ? wr[T < 8 ? T : 0][t] : cat8(lw[2 * (i & 7)], lw[2 * (i & 7) + 1]), acc[t], sidx);
        SB();
        if (i >= 8 && i < 24) { lw[i - 8] = ldsld<v4u>(wl + (i - 8) * 1024); SB(); }
      }
#pragma unroll
    for (int tp = 0; tp < 2; ++tp) ldsst<v2u>(umo + tp * 256, fold_pack(acc[2 * tp], acc[2 * tp + 1]));
    if (w == 1) {
      if (s > 0) {
        const unsigned ref = ck.rte * 4u;
        sc.got_b(stamps_latest(st1, rx, t1o, t1o, pbase, ref, cx.dead));
        sc.got_a(stamps_latest(st2, rx, t2o0, t2o1, sbase, ref, cx.dead));
      }
      ldsst<v2u>(L0 + L2_MISC + 8, (v2u){ck.cycle_of(sc.g2), sc.r1 >= 0 ? 1u : 0u});
    }
    pbase = sbase;
    barx();
    if (w == 3 && s + 1 < NSEG - 1 && !early) { flags_wait(cx, ufo, s + 2); ux_dma(s + 1); }
    if (s < NSEG - 1) {
      if (w < 2) {
        v4u ua[20];
        const v2u gv = ldsld<v2u>(L0 + L2_MISC + 8);
#pragma unroll
        for (int u = 0; u < 16; ++u) ua[u] = ldsld<v4u>(L0 + L2_UX + (s & 1) * 16384 + u * 1024 + lane * 16);
#pragma unroll
        for (int u = 0; u < 4; ++u) ua[16 + u] = ldsld<v4u>(L0 + L2_UM + u * 1024 + lane * 16);
#pragma unroll
        for (int u = 0; u < 20; u += 4) asm volatile("" : "+v"(ua[u]), "+v"(ua[u + 1]), "+v"(ua[u + 2]), "+v"(ua[u + 3]));
        const unsigned g2 = __builtin_amdgcn_readfirstlane(gv.x);
        SB();
        if (__builtin_amdgcn_readfirstlane(gv.y)) wait_cy(g2);
        unsigned ov;
#pragma unroll 1
        for (unsigned spins = 0;; ++spins) {
          v4u pv[10];
#pragma unroll
          for (int u = 0; u < 10; ++u) pv[u] = ld16(rx, po[u], sbase);
          SB();
          f32x4 d0 = Z4, d1 = Z4;
          unsigned m = 0;
#pragma unroll
          for (int u = 0; u < 10; ++u) {
            d0 = mf32(ua[2 * u], pv[u], d0);
            d1 = mf32(ua[2 * u + 1], pv[u], d1);
            m = max(m, umax4(pv[u]));
            SB();
          }
          ov = min(cvtpk(((d0[0] + dppf<0x102>(d0[1])) + dppf<0x104>(d0[2])) + dppf<0x106>(d0[3]),
                         ((d1[0] + dppf<0x102>(d1[1])) + dppf<0x104>(d1[2])) + dppf<0x106>(d1[3])), 0xFFFFFFFEu);
          asm volatile("" ::: "memory");
          if (!__any(m == 0xFFFFFFFFu) || cx.dead) break;
          if ((spins & 63u) == 63u) {
            if (spins > 20000u) __hip_atomic_store(cx.abortw, 1u, RLX_AGENT);
            if (__builtin_amdgcn_readfirstlane(__hip_atomic_load(cx.abortw, RLX_AGENT))) cx.dead = true;
          }
        }
        if ((lane & 6) == 0) st4(ov, rx, ooff, nbase);
        SB();
        if (w == 0) {
          const unsigned cpub = cy32();
          if (lane == 0) st4(min(ck.time_of(cpub), 0xFFFFFFFEu), rx, xmap(XT2 + cg * 16), nbase);
          ldsst<unsigned>(L0 + L2_MISC, cpub);
        }
      }
      barx();
      tref = __builtin_amdgcn_readfirstlane(ldsld<unsigned>(L0 + L2_MISC));
    }
    if (w == 3) {
#pragma unroll
      for (int u = 0; u < 4; ++u) st16(ldsld<v4u>(L0 + L2_UM + u * 1024 + lane * 16), rum, cg * 4096 + u * 1024 + lane * 16, s * (80 * 4096));
      ufv = ld4(cx.rc, ufo, 0);
    }
  }
  if (w == 3) { VMWAIT(0); if (lane == 0) flag_set(cx, C_MF2 + cg * 4, NSEG); }
}

enum { R_K = 0, R_V = 1, R_Q = 2, R_F1 = 3 };
constexpr int RING = 40960, Q_PART = 3 * RING;
constexpr int K_LEAD = 6, U_LEAD = 6, V_LEAD = 9;

__device__ __forceinline__ void load_w192(const u16* W, int ldw, v4u (&wr)[20][3], unsigned L0, int w, int lane) {
  const unsigned trb = (8 * (lane >> 4) + ((lane & 15) >> 2)) * 384 + (3 * w) * 32 + (lane & 3) * 8;
  stage384((const char*)W, ldw * 2, 0, 20, L0, w, lane);
  stage384((const char*)W, ldw * 2, 160, 20, L0 + 61440, w, lane);
#pragma unroll
  for (int c = 0; c < 4; ++c) {
    if (c < 3) VMWAIT(15); else VMWAIT(0);
    barx();
#pragma unroll
    for (int ks = 0; ks < 5; ++ks)
#pragma unroll
      for (int t = 0; t < 3; ++t) { const unsigned ad = L0 + (c & 1) * 61440 + trb + ks * 32 * 384 + t * 32; wr[5 * c + ks][t] = cat(trrd(ad), trrd(ad + 4 * 384)); }
    barx();
    if (c < 2) stage384((const char*)W, ldw * 2, 160 * (c + 2), 20, L0 + (c & 1) * 61440, w, lane);
  }
}
__device__ __forceinline__ void ring_issue(const char* src, int s, unsigned L0, int w) {
  const char* p = src + (size_t)s * RING;
  const unsigned d = L0 + (s % 3) * RING + w * 10240;
  dma16x4(p, d); dma16x4(p + 4096, d + 4096); dma16x2(p + 8192, d + 8192);
}
__device__ __forceinline__ void stagger(int w) {
  for (int i = 0; i < w; ++i) asm volatile("s_nop 7\n\ts_nop 3");
}
template <int OFF> __device__ __forceinline__ void dma16o(const char* gsrc) {
  asm volatile("global_load_lds_dwordx4 %0, off offset:%1 sc1" :: "v"(gsrc), "i"(OFF) : "memory");
}
__device__ __forceinline__ void set_m0(unsigned v) { asm volatile("s_mov_b32 m0, %0\n\ts_nop 0" :: "s"(v) : "memory"); }
__device__ __forceinline__ void gemm192(const v4u (&wr)[20][3], unsigned rb, f32x4 (&acc)[2][3], bool fill, const char* fsrc, unsigned fdst) {
  const f32x4 Z4 = {0.f, 0.f, 0.f, 0.f};
#pragma unroll
  for (int mt = 0; mt < 2; ++mt)
#pragma unroll
    for (int t = 0; t < 3; ++t) acc[mt][t] = Z4;
  stagger(__builtin_amdgcn_readfirstlane(threadIdx.x >> 6));
  v4u hb[4];
  hb[0] = ldsld<v4u>(rb); hb[1] = ldsld<v4u>(rb + 20480); hb[2] = ldsld<v4u>(rb + 1024); hb[3] = ldsld<v4u>(rb + 20480 + 1024);
#pragma unroll
  for (int ks = 0; ks < 20; ++ks)
#pragma unroll
    for (int mt = 0; mt < 2; ++mt) {
      const v4u hv = hb[(2 * ks + mt) & 3];
#pragma unroll
      for (int t = 0; t < 3; ++t) acc[mt][t] = mf32(wr[ks][t], hv, acc[mt][t]);
      SB();
      if (ks + 2 < 20) hb[(2 * ks + mt) & 3] = ldsld<v4u>(rb + mt * 20480 + (ks + 2) * 1024);
      if (mt == 1 && (ks & 1) == 0 && fill) {
        if ((ks & 7) == 0) set_m0(fdst + (ks >> 3) * 4096);
        const char* fp = fsrc + (ks >> 3) * 4096;
        if ((ks & 7) == 0) dma16o<0>(fp); else if ((ks & 7) == 2) dma16o<1024>(fp); else if ((ks & 7) == 4) dma16o<2048>(fp); else dma16o<3072>(fp);
      }
      SB();
    }
}

constexpr int PU_LDS = 3 * RING, PU_MISC = PU_LDS + 32768;
__device__ __forceinline__ void pu_task(const Args& a, const int task, const unsigned L0, Ctx& cx) {
  const int tid = threadIdx.x, lane = tid & 63, w = __builtin_amdgcn_readfirstlane(tid >> 6);
  const rsrc_t rux = mkrs(a.ws + O_UX, (size_t)S * H * DM * 2), rum = mkrs(a.ws + O_UM, (size_t)NSEG * 80 * 4096), rx1 = mkrs(a.ws + O_X1, SZ_ACT);
  const rsrc_t rpx = mkrs(a.ws + O_PX, (size_t)NSEG * 65536), rpm = mkrs(a.ws + O_PM, (size_t)NSEG * 16384);
  const float* mod = (const float*)(a.ws + O_MOD);
  const unsigned P0 = L0 + PU_LDS;
  const f32x4 Z4 = {0.f, 0.f, 0.f, 0.f};
  const int s = task / 10, part = task % 10;
  const unsigned lo = (((lane & 15) >> 3) + part * 8) , li = (lane & 1) * 1024 + (4 * ((lane & 7) >> 1) + (lane >> 4)) * 16;
  const unsigned uxo = lo * 16384 + li + w * 4096, umo = lo * 4096 + li + (w >> 1) * 2048 + (w & 1) * 512;
  const unsigned usb = s * (80 * 16384);
  const int col = part * 64 + w * 16 + 4 * (lane >> 4);
  const f32x4 g = *(const f32x4*)(mod + 2 * DM + col);
  const v2u xr0 = *(const v2u*)(a.x + (size_t)(s * 32 + (lane & 15)) * DM + col), xr1 = *(const v2u*)(a.x + (size_t)(s * 32 + 16 + (lane & 15)) * DM + col);
  v4u u[2][2][4];
#define PU_LDU(buf, k2)                                                                                                     \
  _Pragma("unroll") for (int k = 0; k < 2; ++k) _Pragma("unroll") for (int nt = 0; nt < 4; ++nt)                            \
    u[buf][k][nt] = (k2) < 4 ? ld16(rux, uxo + nt * 32768 + (((k2) * 2 + k) >> 2) * 2048 + (((k2) * 2 + k) & 3) * 256, usb) \
                             : ld16(rum, umo + nt * 8192 + k * 256, s * (80 * 4096));
  v4u p[10][2];
#define PU_LDP(k0, k1)                                                                                                      \
  _Pragma("unroll") for (int k = k0; k < k1; ++k) _Pragma("unroll") for (int mt = 0; mt < 2; ++mt)                          \
    p[k][mt] = k < 8 ? ld16(rpx, (mt * 32 + 8 * w + k) * 1024 + lane * 16, s * 65536) : ld16(rpm, (mt * 8 + 2 * w + k - 8) * 1024 + lane * 16, s * 16384);
  PU_LDP(0, 4)
  flags_wait(cx, lane < 8 ? (C_UF + (8 * w + lane) * 4) * 4 : (C_MF2 + (part * 8 + (lane & 7)) * 4) * 4, s + 1);
  PU_LDU(0, 0) PU_LDU(1, 1) PU_LDP(4, 10)
#undef PU_LDP
  SB();
  f32x4 d[2][4];
#pragma unroll
  for (int mt = 0; mt < 2; ++mt)
#pragma unroll
    for (int nt = 0; nt < 4; ++nt) d[mt][nt] = Z4;
#define PU_MM(buf, k2)                                                                                                      \
  _Pragma("unroll") for (int k = 0; k < 2; ++k) _Pragma("unroll") for (int nt = 0; nt < 4; ++nt) {                          \
    d[0][nt] = mf32(u[buf][k][nt], p[(k2) * 2 + k][0], d[0][nt]); d[1][nt] = mf32(u[buf][k][nt], p[(k2) * 2 + k][1], d[1][nt]); } SB();
  PU_MM(0, 0) PU_LDU(0, 2) SB();
  PU_MM(1, 1) PU_LDU(1, 3) SB();
  PU_MM(0, 2) PU_LDU(0, 4) SB();
  PU_MM(1, 3)
  PU_MM(0, 4)
#undef PU_LDU
#undef PU_MM
#pragma unroll
  for (int mt = 0; mt < 2; ++mt)
#pragma unroll
    for (int nt = 0; nt < 4; ++nt) ldsst<f32x4>(P0 + ((w * 4 + nt) * 2 + mt) * 1024 + lane * 16, d[mt][nt]);
  barx();
#pragma unroll
  for (int mt = 0; mt < 2; ++mt) {
    const unsigned pa = P0 + (w * 2 + mt) * 1024 + lane * 16;
    const f32x4 t = (ldsld<f32x4>(pa) + ldsld<f32x4>(pa + 8192)) + (ldsld<f32x4>(pa + 16384) + ldsld<f32x4>(pa + 24576));
    const v2u xr = mt ? xr1 : xr0;
    st8((v2u){cvtpk(blo(xr.x) + rbf(g[0] * rbf(t[0])), bhi(xr.x) + rbf(g[1] * rbf(t[1]))),
              cvtpk(blo(xr.y) + rbf(g[2] * rbf(t[2])), bhi(xr.y) + rbf(g[3] * rbf(t[3])))}, rx1, ((s * 32 + mt * 16 + (lane & 15)) * DM + col) * 2, 0);
  }
  VMWAIT(0); barx();
  if (tid == 0) ldsst<unsigned>(L0 + PU_MISC, __hip_atomic_fetch_add((gu32*)(a.ws + O_CTL) + C_XC + s * 32, 1u, RLX_AGENT));
  barx();
  if (__builtin_amdgcn_readfirstlane(ldsld<unsigned>(L0 + PU_MISC)) == 9u) {
    ln_segment<true>(a.ws + O_X1 + (size_t)s * 32 * DM * 2, a.ws + O_MODP + 2 * DM * 2, mkrs(a.ws + O_H2X, SZ_ACT), s * RING, w, lane);
    VMWAIT(0); barx();
    if (tid == 0) flag_set(cx, C_HG2 + s * 4, 1u);
  }
}
__device__ __forceinline__ int pu_claim(const Args& a, const unsigned L0, Ctx& cx, int h, int limit, int& seg, int& prog) {
  const int tid = threadIdx.x, lane = tid & 63;
  if (tid < 64) {
    const int sg = min(seg + (lane >> 1), NSEG - 1);
    const unsigned v = ld4(cx.rc, lane < 8 ? (C_PT + sg * 4 + (lane & 1)) * 4 : (C_MF1 + h * 32) * 4, 0);
    const unsigned pr = __builtin_amdgcn_readlane(v, 8) >> 2;
    const unsigned open = (unsigned)__ballot(lane < 8 && !(lane & 1) && v < 10u && seg + (lane >> 1) < NSEG);
    const unsigned rdy = (unsigned)__ballot(lane < 8 && (lane & 1) && v != 0u) >> 1;
    const int k = open ? __builtin_ctz(open) : 8;
    int nseg = seg + (k >> 1), t = nseg >= NSEG ? -2 : (open ? -1 : -3);
    if (open && ((rdy >> k) & 1u) && (int)pr <= limit && lane == 0) {
      const unsigned part = __hip_atomic_fetch_add((gu32*)(a.ws + O_CTL) + C_PT + nseg * 4, 1u, RLX_AGENT);
      if (part < 10u) t = nseg * 10 + (int)part; else t = -3;
    }
    if (lane == 0) ldsst<v4u>(L0 + PU_MISC + 32, (v4u){(unsigned)t, pr, (unsigned)nseg, 0u});
  }
  barx();
  const v4u r = ldsld<v4u>(L0 + PU_MISC + 32);
  prog = __builtin_amdgcn_readfirstlane(r.y);
  seg = __builtin_amdgcn_readfirstlane(r.z);
  const int t = (int)__builtin_amdgcn_readfirstlane(r.x);
  barx();
  return t;
}
__device__ __forceinline__ void gap_work(const Args& a, const unsigned L0, Ctx& cx, int h, int s, int lead, int& seg) {
#pragma unroll 1
  for (int it = 0; it < 60000; ++it) {
    int prog;
    const int t = pu_claim(a, L0, cx, h, s - lead, seg, prog);
    if (t >= 0) { pu_task(a, t, L0, cx); continue; }
    if (t == -3) continue;
    if (prog + lead > s || (t == -2 && s >= NSEG)) break;
    __builtin_amdgcn_s_sleep(10);
    if ((it & 255) == 255 && __builtin_amdgcn_readfirstlane(__hip_atomic_load(cx.abortw, RLX_AGENT))) break;
  }
}
__device__ __forceinline__ bool is_ahead(unsigned mnow, int s, int lead, const unsigned L0) {
  if (threadIdx.x == 0) ldsst<unsigned>(L0 + PU_MISC + 16, mnow);
  barx();
  return (int)(__builtin_amdgcn_readfirstlane(ldsld<unsigned>(L0 + PU_MISC + 16)) >> 2) + lead <= s;
}

template <int ROLE>
__device__ __forceinline__ void stream_kv(const Args& a, const int u, const unsigned L0, Ctx& cx) {
  const int tid = threadIdx.x, lane = tid & 63, w = __builtin_amdgcn_readfirstlane(tid >> 6);
  constexpr int LEAD = ROLE == R_K ? K_LEAD : V_LEAD;
  v4u wr[20][3];
  load_w192((ROLE == R_K ? a.Wk : a.Wv) + u * DH, HD, wr, L0, w, lane);
  const char* src = a.ws + O_HX + w * 10240 + lane * 16;
  const rsrc_t rdst = mkrs(a.ws + (ROLE == R_K ? O_KX : O_VX), (size_t)S * HD * 2);
  const unsigned fword = (ROLE == R_K ? C_KF : C_VF) + u * 32;
  const char* mfp = a.ws + O_CTL + (C_MF1 + u * 32) * 4;
  STAMP(cx.slot, 1);
  int known = 0;
  seg_wait<C_HF1>(cx, 0, known, lane); ring_issue(src, 0, L0, w);
  seg_wait<C_HF1>(cx, 1, known, lane); ring_issue(src, 1, L0, w);
  bool drained = true;
  unsigned mprev = 0;
#pragma unroll 1
  for (int s = 0; s < NSEG; ++s) {
    if (s == 0) VMWAIT(10);
    else if (drained || s == NSEG - 1) VMWAIT(0);
    else if (ROLE == R_K) VMWAIT(13);
    else VMWAIT(14);
    PIN(mprev);
    if (tid == 0) ldsst<unsigned>(L0 + PU_MISC + 16 + (s & 1) * 4, mprev);
    barx();
    if (!drained && tid == 0) flag_set(cx, fword, s == NSEG - 1 ? s : s - 1);
    const bool ahead = (int)(__builtin_amdgcn_readfirstlane(ldsld<unsigned>(L0 + PU_MISC + 16 + (s & 1) * 4)) >> 2) + LEAD <= s;
    if (s + 2 < NSEG) seg_wait<C_HF1>(cx, s + 2, known, lane);
    mprev = ld4_async(mfp);
    f32x4 acc[2][3];
    gemm192(wr, L0 + (s % 3) * RING + lane * 16, acc, s + 2 < NSEG, src + (size_t)(s + 2) * RING, L0 + ((s + 2) % 3) * RING + w * 10240);
    const unsigned sb = s * (H * 12288);
    if (ROLE == R_K) {
      const unsigned o = (u * 4 + w) * 3072 + lane * 16;
      st16(cat(pack4(acc[0][0]), pack4(acc[0][1])), rdst, o, sb);
      st16(cat(pack4(acc[1][0]), pack4(acc[1][1])), rdst, o + 1024, sb);
      st16(cat(pack4(acc[0][2]), pack4(acc[1][2])), rdst, o + 2048, sb);
    } else {
      const unsigned o = u * 12288 + lane * 16;
#pragma unroll
      for (int mt = 0; mt < 2; ++mt) {
        st16(cat(pack4(acc[mt][0]), pack4(acc[mt][1])), rdst, o + mt * 6144 + w * 1024, sb);
        st8(pack4(acc[mt][2]), rdst, o + mt * 6144 + (4 + (w >> 1)) * 1024 + (w & 1) * 8, sb);
      }
    }
    drained = s < 2 || s == NSEG - 1 || ahead;
    if (drained) {
      VMWAIT(0); barx();
      if (tid == 0) flag_set(cx, fword, s + 1);
    }
    STAMP(cx.slot, 8 + s);
    if (ahead && s < NSEG - 1) { PIN(mprev); gap_work(a, L0, cx, u, s, LEAD, cx.seg); mprev = 0xFFFFu; }
  }
}

__device__ __forceinline__ void stream_f1(const Args& a, const int u, const unsigned L0, Ctx& cx) {
  const int tid = threadIdx.x, lane = tid & 63, w = __builtin_amdgcn_readfirstlane(tid >> 6);
  v4u wr[20][3];
  load_w192(a.w1 + u * DH, FF, wr, L0, w, lane);
  const char* src = a.ws + O_H2X + w * 10240 + lane * 16;
  const char* hfp = a.ws + O_CTL + (C_HG2 + lane * 4) * 4;
  const rsrc_t rdst = mkrs(a.ws + O_FX, (size_t)S * FF * 2);
  float bias[3][4];
#pragma unroll
  for (int t = 0; t < 3; ++t)
#pragma unroll
    for (int r = 0; r < 4; ++r) bias[t][r] = bf2f(a.b1[u * DH + 16 * (3 * w + t) + 4 * (lane >> 4) + r]);
  STAMP(cx.slot, 1);
  int issued = 0, known = 0;
  bool owe = false;
#pragma unroll 1
  for (int s = 0; s < NSEG; ++s) {
    if (issued == s) { seg_wait<C_HG2>(cx, s, known, lane); ring_issue(src, s, L0, w); issued = s + 1; VMWAIT(0); }
    barx();
    const bool fill = issued < known && issued <= s + 2;
    unsigned fv = ld4_async(hfp);
    f32x4 acc[2][3];
    gemm192(wr, L0 + (s % 3) * RING + lane * 16, acc, fill, src + (size_t)issued * RING, L0 + (issued % 3) * RING + w * 10240);
    if (fill) ++issued;
#pragma unroll
    for (int mt = 0; mt < 2; ++mt)
#pragma unroll
      for (int t = 0; t < 3; ++t)
#pragma unroll
        for (int r = 0; r < 4; r += 2) {
          const unsigned p = cvtpk(acc[mt][t][r], acc[mt][t][r + 1]);
          const unsigned q = cvtpk(blo(p) + bias[t][r], bhi(p) + bias[t][r + 1]);
          const float v0 = __builtin_amdgcn_fmed3f(blo(q), 0.f, 3.4e38f), v1 = __builtin_amdgcn_fmed3f(bhi(q), 0.f, 3.4e38f);
          acc[mt][t][r] = v0 * v0; acc[mt][t][r + 1] = v1 * v1;
        }
    VMWAIT(0);
    PIN(fv);
    if (owe && lane == 0) __hip_atomic_fetch_add((gu32*)(a.ws + O_CTL) + C_FF + u * 4, 1u, RLX_AGENT);
    { const unsigned long long miss = __ballot(fv == 0u); known = miss ? __builtin_ctzll(miss) : 64; }
    if (issued == s + 1 && issued < known) { ring_issue(src, issued, L0, w); ++issued; }
    const unsigned o = u * 12288 + lane * 16, sb = s * (8 * 12288);
#pragma unroll
    for (int mt = 0; mt < 2; ++mt) {
      st16(cat(pack4(acc[mt][0]), pack4(acc[mt][1])), rdst, o + mt * 6144 + w * 1024, sb);
      st8(pack4(acc[mt][2]), rdst, o + mt * 6144 + (4 + (w >> 1)) * 1024 + (w & 1) * 8, sb);
    }
    owe = fill && issued > s + 1;
    if (!owe) {
      VMWAIT(0);
      if (lane == 0) __hip_atomic_fetch_add((gu32*)(a.ws + O_CTL) + C_FF + u * 4, 1u, RLX_AGENT);
    }
    STAMP(cx.slot, 8 + s);
  }
}

__device__ __forceinline__ void q_publish(const Args& a, int s, int lane) {
  if (lane == 0) {
    gu32* c = (gu32*)(a.ws + O_CTL);
    if (__hip_atomic_fetch_add(c + C_XC + s * 32 + 16, 1u, RLX_AGENT) == 127u) __hip_atomic_store(c + C_PT + s * 4 + 1, 1u, RLX_AGENT);
  }
}
__device__ __forceinline__ void stream_q(const Args& a, const int u, const unsigned L0, Ctx& cx) {
  const int tid = threadIdx.x, lane = tid & 63, w = __builtin_amdgcn_readfirstlane(tid >> 6);
  v4u wr[20][3];
  load_w192(a.Wq + u * DH, HD, wr, L0, w, lane);
  const char* src = a.ws + O_HX + w * 10240 + lane * 16;
  const rsrc_t rpx = mkrs(a.ws + O_PX, (size_t)NSEG * 65536), rpm = mkrs(a.ws + O_PM, (size_t)NSEG * 16384);
  const rsrc_t rk = mkrs(a.ws + O_KX, (size_t)S * HD * 2), rkm = mkrs(a.ws + O_KM, (size_t)NSEG * H * 6144);
  const unsigned kfo = (C_KF + u * 32) * 4, mfo = (C_MF1 + u * 32) * 4, ko = (u * 4 + w) * 3072 + lane * 16, mo = (u * 4 + w) * 1536;
  const unsigned fo = (lane & 1) ? kfo : mfo;
  const int smt = w >> 1, g = lane & 7, c16 = 8 * (w & 1) + (lane >> 3), last = 16 * smt + c16 - 4 * g;
  const unsigned pbuf = L0 + Q_PART;
  const unsigned rxa = pbuf + (smt * 3 + (g >> 2)) * 1024 + ((g & 3) * 16 + c16) * 16;
  const unsigned rma = pbuf + (smt * 3 + 2) * 1024 + ((2 + (g >> 2)) * 16 + c16) * 16 + (g & 3) * 4;
  const unsigned sxo = (smt * 32 + u) * 1024 + ((g & 3) * 16 + c16) * 16 + (g >> 2) * 8;
  const unsigned smo = (smt * 8 + (u >> 2)) * 1024 + ((u & 3) * 16 + c16) * 16 + g * 2;
  const f32x4 Z4 = {0.f, 0.f, 0.f, 0.f};
  STAMP(cx.slot, 1);
  int known = 0;
  seg_wait<C_HF1>(cx, 2, known, lane);
  ring_issue(src, 0, L0, w); ring_issue(src, 1, L0, w);
  v4u q01[2]; v2u q2[2];
  unsigned kready = 0, mready = 0, fv = 0;
  bool owe = false;
#pragma unroll 1
  for (int s = -1; s < NSEG; ++s) {
    v4u k0, k1, k2, m01; v2u m2;
    const bool early = s >= 0 && mready >= 4u * (s + 1);
    if (s >= 0) {
      if (kready < (unsigned)s + 1u) flags_wait(cx, kfo, s + 1);
      k0 = ld16(rk, ko, s * (H * 12288)); k1 = ld16(rk, ko + 1024, s * (H * 12288)); k2 = ld16(rk, ko + 2048, s * (H * 12288));
      if (early) { m01 = ld16(rkm, mo + lane * 16, s * (H * 6144)); m2 = ld8(rkm, mo + 1024 + lane * 8, s * (H * 6144)); }
    }
    v4u n01[2]; v2u n2[2];
    if (s + 1 < NSEG) {
      if (s < 0) VMWAIT(10);
      barx();
      if (s + 3 < NSEG) seg_wait<C_HF1>(cx, s + 3, known, lane);
      f32x4 acc[2][3];
      gemm192(wr, L0 + ((s + 1) % 3) * RING + lane * 16, acc, s + 3 < NSEG, src + (size_t)(s + 3) * RING, L0 + ((s + 3) % 3) * RING + w * 10240);
      fv = ld4_async(a.ws + O_CTL + fo);
#pragma unroll
      for (int mt = 0; mt < 2; ++mt) { n01[mt] = cat(pack4(acc[mt][0]), pack4(acc[mt][1])); n2[mt] = pack4(acc[mt][2]); }
    }
    VMWAIT(0);
    PIN(fv);
    mready = __builtin_amdgcn_readlane(fv, 0); kready = __builtin_amdgcn_readlane(fv, 1);
    if (owe) q_publish(a, s - 1, lane);
    owe = false;
    if (s >= 0) {
      if (!early) {
        if (mready < 4u * (s + 1)) flags_wait(cx, mfo, 4 * (s + 1));
        m01 = ld16(rkm, mo + lane * 16, s * (H * 6144)); m2 = ld8(rkm, mo + 1024 + lane * 8, s * (H * 6144));
      }
      const v4u qa0 = {q2[0].x, q2[0].y, 0u, 0u}, qa1 = {q2[1].x, q2[1].y, 0u, 0u}, qb1 = {0u, 0u, q2[1].x, q2[1].y}, mz = {m2.x, m2.y, 0u, 0u};
      f32x4 t0 = mf32(k0, q01[0], Z4), t1 = mf32(k0, q01[1], Z4), t2 = mf32(k1, q01[1], Z4), t3 = mf32(m01, q01[0], Z4), t4 = mf32(m01, q01[1], Z4);
      t0 = mf32(k2, qa0, t0); t1 = mf32(k2, qa1, t1); t2 = mf32(k2, qb1, t2); t3 = mf32(mz, qa0, t3); t4 = mf32(mz, qa1, t4);
      const unsigned pa = pbuf + w * 6144 + lane * 16;
      ldsst<f32x4>(pa, t0); ldsst<f32x4>(pa + 3072, t1); ldsst<f32x4>(pa + 4096, t2); ldsst<f32x4>(pa + 2048, t3); ldsst<f32x4>(pa + 5120, t4);
      barx();
      const f32x4 xs = (ldsld<f32x4>(rxa) + ldsld<f32x4>(rxa + 6144)) + (ldsld<f32x4>(rxa + 12288) + ldsld<f32x4>(rxa + 18432));
      const float ms = (ldsld<float>(rma) + ldsld<float>(rma + 6144)) + (ldsld<float>(rma + 12288) + ldsld<float>(rma + 18432));
      const unsigned a0 = cvtpk(xs[0], xs[1]), a1 = cvtpk(xs[2], xs[3]), a2 = cvtpk(ms, ms);
      const unsigned b0 = cvtpk(blo(a0) * SCALE, bhi(a0) * SCALE), b1 = cvtpk(blo(a1) * SCALE, bhi(a1) * SCALE), b2 = cvtpk(blo(a2) * SCALE, 0.f);
      const float NEG = -3.0e38f;
      const float v0 = last >= 0 ? blo(b0) : NEG, v1 = last >= 1 ? bhi(b0) : NEG, v2 = last >= 2 ? blo(b1) : NEG, v3 = last >= 3 ? bhi(b1) : NEG, v4 = blo(b2);
      float mx = __builtin_amdgcn_fmed3f(__builtin_amdgcn_fmed3f(v0, v1, 3.4e38f), __builtin_amdgcn_fmed3f(v2, v3, 3.4e38f), 3.4e38f);
      mx = __builtin_amdgcn_fmed3f(mx, v4, 3.4e38f);
      mx = __builtin_amdgcn_fmed3f(mx, dppf<0xB1>(mx), 3.4e38f);
      mx = __builtin_amdgcn_fmed3f(mx, dppf<0x4E>(mx), 3.4e38f);
      mx = __builtin_amdgcn_fmed3f(mx, dppf<0x141>(mx), 3.4e38f);
      const float e0 = __builtin_amdgcn_exp2f((v0 - mx) * LOG2E), e1 = __builtin_amdgcn_exp2f((v1 - mx) * LOG2E), e2 = __builtin_amdgcn_exp2f((v2 - mx) * LOG2E),
                  e3 = __builtin_amdgcn_exp2f((v3 - mx) * LOG2E), e4 = __builtin_amdgcn_exp2f((v4 - mx) * LOG2E);
      float sum = ((e0 + e1) + (e2 + e3)) + e4;
      sum += dppf<0xB1>(sum); sum += dppf<0x4E>(sum); sum += dppf<0x141>(sum);
      const float inv = __builtin_amdgcn_rcpf(sum);
      st8((v2u){cvtpk(e0 * inv, e1 * inv), cvtpk(e2 * inv, e3 * inv)}, rpx, sxo, s * 65536);
      const float pm = e4 * inv, po = dppf<0xB1>(pm);
      if (!(g & 1)) st4(cvtpk(pm, po), rpm, smo, s * 16384);
      if (mready >= 4u * (s + 2) && s + 1 < NSEG) owe = true;
      else {
        VMWAIT(0);
        q_publish(a, s, lane);
      }
      if (s + 1 >= NSEG - 1) barx();
      STAMP(cx.slot, 8 + s);
    }
#pragma unroll
    for (int mt = 0; mt < 2; ++mt) { q01[mt] = n01[mt]; q2[mt] = n2[mt]; }
  }
}

__device__ __forceinline__ void unit_u(const Args& a, const int h, const unsigned L0, Ctx& cx) {
  const int tid = threadIdx.x, lane = tid & 63, w = __builtin_amdgcn_readfirstlane(tid >> 6);
  v2u wh[12][10];
  {
    const char* W = (const char*)(a.Wo + (size_t)h * DH * DM);
    const unsigned trb = (4 * (lane >> 4) + ((lane & 15) >> 2)) * 1280 + w * 320 + (lane & 3) * 8;
    auto stage = [&](int c) {
#pragma unroll 1
      for (int i = w; i < 60; i += 4) dma16p(W + c * 61440 + i * 1024 + lane * 16, L0 + (c & 1) * 61440 + i * 1024);
    };
    stage(0); stage(1);
#pragma unroll
    for (int c = 0; c < 4; ++c) {
      if (c < 3) VMWAIT(15); else VMWAIT(0);
      barx();
#pragma unroll
      for (int t = 0; t < 3; ++t)
#pragma unroll
        for (int nt = 0; nt < 10; ++nt) wh[3 * c + t][nt] = trrd(L0 + (c & 1) * 61440 + trb + t * 16 * 1280 + nt * 32);
      barx();
      if (c < 2) stage(c + 2);
    }
  }
  const char* src = a.ws + O_VX + (size_t)h * 12288 + w * 3072 + lane * 16;
  const rsrc_t rdst = mkrs(a.ws + O_UX, (size_t)S * H * DM * 2);
  const unsigned vfo = (C_VF + h * 32) * 4;
  const unsigned so = ((lane & 15) >> 3) * 16384 + (h >> 2) * 2048 + (lane & 1) * 1024 + ((h & 3) * 16 + 4 * ((lane & 7) >> 1) + (lane >> 4)) * 16 + w * (20 * 16384);
  auto issue = [&](int s) {
    dma16x2(src + (size_t)s * (H * 12288), L0 + (s & 3) * 12288 + w * 3072);
    dma16(src + (size_t)s * (H * 12288) + 2048, L0 + (s & 3) * 12288 + w * 3072 + 2048);
  };
  STAMP(cx.slot, 1);
  int issued = 0;
  bool drained = true;
  unsigned mprev = 0;
  const f32x4 Z4 = {0.f, 0.f, 0.f, 0.f};
#pragma unroll 1
  for (int s = 0; s < NSEG; ++s) {
    if (issued == s) { flags_wait(cx, vfo, s + 1); issue(s); issued = s + 1; VMWAIT(0); }
    else if (drained) VMWAIT(0);
    else VMWAIT(5);
    PIN(mprev);
    if (tid == 0) ldsst<unsigned>(L0 + PU_MISC + 16 + (s & 1) * 4, mprev);
    barx();
    if (!drained && tid == 0) flag_set(cx, C_UF + h * 4, s - 1);
    const bool ahead = (int)(__builtin_amdgcn_readfirstlane(ldsld<unsigned>(L0 + PU_MISC + 16 + (s & 1) * 4)) >> 2) + U_LEAD <= s;
    mprev = ld4_async(a.ws + O_CTL + (C_MF1 + h * 32) * 4);
    const unsigned rb = L0 + (s & 3) * 12288 + lane * 16;
    v4u vf[2][6];
#pragma unroll
    for (int mt = 0; mt < 2; ++mt)
#pragma unroll
      for (int ks = 0; ks < 6; ++ks) vf[mt][ks] = ldsld<v4u>(rb + (mt * 6 + ks) * 1024);
    const unsigned vnow = ld4(cx.rc, vfo, 0);
#pragma unroll
    for (int nt = 0; nt < 10; ++nt) {
      f32x4 d0 = Z4, d1 = Z4;
#pragma unroll
      for (int ks = 0; ks < 6; ++ks) {
        const v4u b = cat(wh[ks < 4 ? 3 * ks : (ks == 4 ? 2 : 8)][nt], wh[ks < 4 ? 3 * ks + 1 : (ks == 4 ? 5 : 11)][nt]);
        d0 = mf32(vf[0][ks], b, d0);
        d1 = mf32(vf[1][ks], b, d1);
      }
      st16(cat(pack4(d0), pack4(d1)), rdst, so + nt * 32768, s * (80 * 16384));
      if (nt == 4 && s + 1 < NSEG && (unsigned)__builtin_amdgcn_readfirstlane(vnow) >= (unsigned)s + 2u) { issue(s + 1); issued = s + 2; }
    }
    drained = s < 2 || s == NSEG - 1 || ahead;
    if (drained) {
      VMWAIT(0); barx();
      if (tid == 0) flag_set(cx, C_UF + h * 4, s + 1);
    }
    STAMP(cx.slot, 8 + s);
    if (ahead && s < NSEG - 1) { PIN(mprev); gap_work(a, L0, cx, h, s, U_LEAD, cx.seg); mprev = 0xFFFFu; }
  }
}

__device__ __forceinline__ void unit_f2(const Args& a, const int jj, const unsigned L0, Ctx& cx) {
  const int tid = threadIdx.x, lane = tid & 63, w = __builtin_amdgcn_readfirstlane(tid >> 6);
  v2u wh[2][12][5];
  {
    const unsigned trb = w * 30720 + (4 * (lane >> 4) + ((lane & 15) >> 2)) * 160 + (lane & 3) * 8;
#pragma unroll
    for (int c = 0; c < 2; ++c) {
      for (int i = 0; i < 30; ++i) {
        const unsigned idx = i * 64 + lane, row = idx / 10, ch = idx % 10;
        dma16p(a.w2 + (size_t)(384 * w + 192 * c + row) * DM + jj * 80 + ch * 8, L0 + w * 30720 + i * 1024);
      }
      VMWAIT(0); barx();
#pragma unroll
      for (int t = 0; t < 12; ++t)
#pragma unroll
        for (int nt = 0; nt < 5; ++nt) wh[c][t][nt] = trrd(L0 + trb + t * 16 * 160 + nt * 32);
      barx();
    }
  }
  const rsrc_t rf = mkrs(a.ws + O_FX, (size_t)S * FF * 2), rx1 = mkrs(a.ws + O_X1, SZ_ACT);
  const unsigned ffo = (C_FF + (2 * w + (lane & 1)) * 4) * 4, fo = 2 * w * 12288 + lane * 16;
  v2u bb[3]; f32x4 gg[3]; unsigned oo[3];
#pragma unroll
  for (int k = 0; k < 3; ++k) {
    const int i = w + 4 * k < 10 ? w + 4 * k : w, col = jj * 80 + (i % 5) * 16 + 4 * (lane >> 4);
    bb[k] = *(const v2u*)(a.b2 + col);
    gg[k] = *(const f32x4*)((const float*)(a.ws + O_MOD) + 5 * DM + col);
    oo[k] = (((i / 5) * 16 + (lane & 15)) * DM + col) * 2;
  }
  STAMP(cx.slot, 1);
  const f32x4 Z4 = {0.f, 0.f, 0.f, 0.f};
  v4u fa[12], fb[12];
  bool have = false;
#pragma unroll 1
  for (int s = 0; s < NSEG; ++s) {
    const unsigned sb = s * (8 * 12288);
    if (!have) {
      flags_wait(cx, ffo, 4 * (s + 1));
#pragma unroll
      for (int i = 0; i < 12; ++i) fa[i] = ld16(rf, fo + i * 1024, sb);
    }
    const unsigned nf = ld4(cx.rc, ffo, 0);
    v2u xr[3];
    f32x4 acc[2][5];
#pragma unroll
    for (int mt = 0; mt < 2; ++mt)
#pragma unroll
      for (int nt = 0; nt < 5; ++nt) acc[mt][nt] = Z4;
    SB();
#pragma unroll
    for (int ks = 0; ks < 6; ++ks)
#pragma unroll
      for (int nt = 0; nt < 5; ++nt) {
        const v4u wa = cat(wh[0][ks < 4 ? 3 * ks : (ks == 4 ? 2 : 8)][nt], wh[0][ks < 4 ? 3 * ks + 1 : (ks == 4 ? 5 : 11)][nt]);
        acc[0][nt] = mf32(wa, fa[ks], acc[0][nt]);
        acc[1][nt] = mf32(wa, fa[6 + ks], acc[1][nt]);
        SB();
        if (nt == 0) fb[ks] = ld16(rf, fo + 12288 + ks * 1024, sb);
        if (nt == 2) fb[6 + ks] = ld16(rf, fo + 12288 + (6 + ks) * 1024, sb);
        if (nt == 4 && ks < 3) xr[ks] = ld8(rx1, oo[ks], s * (32 * DM * 2));
        SB();
      }
    have = s + 1 < NSEG && !__any(nf < 4u * (s + 2));
    const unsigned nsb = have ? sb + 8 * 12288 : sb;
    SB();
#pragma unroll
    for (int ks = 0; ks < 6; ++ks)
#pragma unroll
      for (int nt = 0; nt < 5; ++nt) {
        const v4u wa = cat(wh[1][ks < 4 ? 3 * ks : (ks == 4 ? 2 : 8)][nt], wh[1][ks < 4 ? 3 * ks + 1 : (ks == 4 ? 5 : 11)][nt]);
        acc[0][nt] = mf32(wa, fb[ks], acc[0][nt]);
        acc[1][nt] = mf32(wa, fb[6 + ks], acc[1][nt]);
        SB();
        if (nt == 1) fa[ks] = ld16(rf, fo + ks * 1024, nsb);
        if (nt == 3) fa[6 + ks] = ld16(rf, fo + (6 + ks) * 1024, nsb);
        SB();
      }
    const unsigned pb = L0 + (s & 1) * 40960;
#pragma unroll
    for (int mt = 0; mt < 2; ++mt)
#pragma unroll
      for (int nt = 0; nt < 5; ++nt) ldsst<f32x4>(pb + (w * 10 + mt * 5 + nt) * 1024 + lane * 16, acc[mt][nt]);
    barx();
#pragma unroll
    for (int k = 0; k < 3; ++k) {
      if (w + 4 * k < 10) {
        const unsigned pa = pb + (w + 4 * k) * 1024 + lane * 16;
        const f32x4 t = (ldsld<f32x4>(pa) + ldsld<f32x4>(pa + 10240)) + (ldsld<f32x4>(pa + 20480) + ldsld<f32x4>(pa + 30720));
        const float o0 = blo(xr[k].x) + rbf(gg[k][0] * rbf(rbf(t[0]) + blo(bb[k].x))), o1 = bhi(xr[k].x) + rbf(gg[k][1] * rbf(rbf(t[1]) + bhi(bb[k].x)));
        const float o2 = blo(xr[k].y) + rbf(gg[k][2] * rbf(rbf(t[2]) + blo(bb[k].y))), o3 = bhi(xr[k].y) + rbf(gg[k][3] * rbf(rbf(t[3]) + bhi(bb[k].y)));
        *(v2u*)((char*)a.out + (size_t)s * (32 * DM * 2) + oo[k]) = (v2u){cvtpk(o0, o1), cvtpk(o2, o3)};
      }
    }
    STAMP(cx.slot, 8 + s);
  }
}

__global__ void __launch_bounds__(256, 1) k_main(Args a) {
  extern __shared__ __attribute__((aligned(16))) char lds_[];
  const unsigned L0 = (unsigned)(size_t)(LAS char*)lds_;
  const int tid = threadIdx.x, lane = tid & 63, w = __builtin_amdgcn_readfirstlane(tid >> 6);
  if (tid == 0) {
    unsigned x = xcc_id() & 7u, slot = 0;
    for (int tries = 0; tries < 8; ++tries) {
      const unsigned rk = __hip_atomic_fetch_add((gu32*)(a.ws + O_CTL) + C_TICK + x * 32, 1u, RLX_AGENT);
      if (rk < 32u) { slot = x * 32u + rk; break; }
      x = (x + 1u) & 7u;
    }
    ldsst<unsigned>(L0, slot);
  }
  barx();
  const unsigned slot = __builtin_amdgcn_readfirstlane(ldsld<unsigned>(L0));
  barx();
  Ctx cx{mkrs(a.ws + O_CTL, 65536), (gu32*)(a.ws + O_CTL) + C_ABORT, false, slot, 0};
  STAMP(slot, 0);
  const unsigned x = slot >> 5, rk = slot & 31u;
  if (x < 4 && rk < 8) { layer1(a, x * 8 + rk, L0, cx); STAMP(slot, 2); barx(); gap_work(a, L0, cx, 0, 1000, 0, cx.seg); STAMP(slot, 3); return; }
  if (x < 4 && rk < 28) { layer2(a, x * 20 + (rk - 8), L0, cx); STAMP(slot, 2); barx(); gap_work(a, L0, cx, 0, 1000, 0, cx.seg); STAMP(slot, 3); return; }
  auto ln1 = [&](int seg) {
    ln_segment<false>((const char*)(a.x + (size_t)seg * 32 * DM), a.ws + O_MODP, mkrs(a.ws + O_HX, SZ_ACT), seg * RING, w, lane);
    VMWAIT(0); barx();
    if (tid == 0) flag_set(cx, C_HF1 + seg * 4, 1u);
  };
  if (x < 4) {
    const int f = x * 4 + (rk - 28);
    ln1(32 + 2 * f); ln1(33 + 2 * f);
    flags_wait(cx, C_UF * 4, 1u);
    if (f < 8) stream_f1(a, f, L0, cx); else unit_f2(a, f - 8, L0, cx);
    STAMP(slot, 2); STAMP(slot, 3);
    return;
  }
  const int h = (x - 4) * 8 + (rk & 7), kind = rk >> 3;
  if (kind == 3) { ln1(h); flags_wait(cx, (C_VF + h * 32) * 4, 1u); }
  if (kind == 0) stream_kv<R_K>(a, h, L0, cx);
  else if (kind == 1) stream_kv<R_V>(a, h, L0, cx);
  else if (kind == 2) unit_u(a, h, L0, cx);
  else { stream_q(a, h, L0, cx); STAMP(slot, 2); STAMP(slot, 3); return; }
  STAMP(slot, 2);
  barx();
  gap_work(a, L0, cx, h, 1000, 0, cx.seg);
  STAMP(slot, 3);
}

extern "C" void kernel_launch(void* const* d_in, const int* in_sizes, int n_in, void* d_out, int out_size, void* d_ws, size_t ws_size, hipStream_t stream) {
  if (ws_size < WS_NEED) return;
  Args a;
  a.x = (const u16*)d_in[0]; a.mem0 = (const u16*)d_in[1]; a.Wq = (const u16*)d_in[2]; a.Wk = (const u16*)d_in[3]; a.Wv = (const u16*)d_in[4]; a.Wo = (const u16*)d_in[5];
  a.w1 = (const u16*)d_in[8]; a.b1 = (const u16*)d_in[9]; a.w2 = (const u16*)d_in[10]; a.b2 = (const u16*)d_in[11]; a.c = (const u16*)d_in[12]; a.wmod = (const u16*)d_in[13];
  a.ws = (char*)d_ws; a.out = (u16*)d_out;
  a.w1q = 80; a.w2q = 90;
  (void)hipFuncSetAttribute((const void*)k_main, hipFuncAttributeMaxDynamicSharedMemorySize, MAIN_LDS);
  hipLaunchKernelGGL(k_pre, dim3(PRE_GRID), dim3(256), 0, stream, a);
  hipLaunchKernelGGL(k_main, dim3(256), dim3(256), MAIN_LDS, stream, a);
}
```

```cpp
#include <hip/hip_runtime.h>
#include <hip/hip_bf16.h>
#include <stdint.h>
#include <stdlib.h>

typedef unsigned short u16;
typedef __bf16 bf16x2 __attribute__((ext_vector_type(2)));
typedef __bf16 bf16x4 __attribute__((ext_vector_type(4)));
typedef __bf16 bf16x8 __attribute__((ext_vector_type(8)));
typedef __bf16 bf16x16 __attribute__((ext_vector_type(16)));
typedef float f32x2 __attribute__((ext_vector_type(2)));
typedef float f32x4 __attribute__((ext_vector_type(4)));
typedef unsigned v2u __attribute__((ext_vector_type(2)));
typedef unsigned v4u __attribute__((ext_vector_type(4)));
typedef unsigned v8u __attribute__((ext_vector_type(8)));
#define GAS __attribute__((address_space(1)))
#define LAS __attribute__((address_space(3)))
typedef GAS unsigned gu32;
#define RLX_AGENT __ATOMIC_RELAXED, __HIP_MEMORY_SCOPE_AGENT
typedef __amdgpu_buffer_rsrc_t rsrc_t;

constexpr int S = 2048, DM = 640, H = 32, DH = 192, HD = H * DH, NSEG = 64, FF = 1536;
constexpr float SCALE = 0.07216878364870322f, LOG2E = 1.4426950408889634f;

constexpr unsigned XSTEP = 131072;
constexpr unsigned XP = 10240, XT1 = 30720, XT2 = 31232;
__host__ __device__ constexpr unsigned xmap(unsigned o) { return ((o >> 13) << 14) + (o & 8191u); }
constexpr size_t O_XR = 0, O_CTL = O_XR + (size_t)NSEG * XSTEP, O_SEL = O_CTL + 65536, O_MOD = O_SEL + 4096, O_MODP = O_MOD + 16384, O_HX = O_MODP + 8192;
constexpr size_t SZ_ACT = (size_t)S * DM * 2;
constexpr size_t O_H2X = O_HX + SZ_ACT, O_X1 = O_H2X + SZ_ACT, O_KX = O_X1 + SZ_ACT, O_VX = O_KX + (size_t)S * HD * 2, O_UX = O_VX + (size_t)S * HD * 2;
constexpr size_t O_UM = O_UX + (size_t)S * H * DM * 2, O_KM = O_UM + (size_t)NSEG * 80 * 4096, O_PX = O_KM + (size_t)NSEG * H * 6144;
constexpr size_t O_PM = O_PX + (size_t)NSEG * 65536, O_FX = O_PM + (size_t)NSEG * 16384, O_BT = O_FX + (size_t)S * FF * 2, O_DBG = O_BT + (size_t)H * DM * DM * 2;
constexpr size_t WS_NEED = O_DBG + (4u << 20);
constexpr int C_TICK = 0, C_ABORT = 256, C_LNC = 288, C_KF = 512, C_VF = 1536, C_MF1 = 2560, C_UF = 3584, C_PF = 3712, C_MF2 = 3840, C_XC = 4224,
              C_HF2 = 6272, C_FF = 8320, C_PUT = 8416, C_HF1 = 8448, C_HG2 = 8704, C_PT = 8960, C_SCH = 9216, C_WORDS = 9248;

struct Args {
  const u16 *x, *mem0, *Wq, *Wk, *Wv, *Wo, *w1, *b1, *w2, *b2, *c, *wmod;
  char* ws; u16* out;
  int w1q, w2q, l2d, lk, lu, lv;
};

__device__ __forceinline__ float bf2f(u16 v) { return __uint_as_float(((unsigned)v) << 16); }
__device__ __forceinline__ float blo(unsigned v) { return __uint_as_float(v << 16); }
__device__ __forceinline__ float bhi(unsigned v) { return __uint_as_float(v & 0xFFFF0000u); }
__device__ __forceinline__ unsigned cvtpk(float a, float b) { return __builtin_bit_cast(unsigned, __builtin_convertvector((f32x2){a, b}, bf16x2)); }
__device__ __forceinline__ float rbf(float f) { return blo(cvtpk(f, f)); }
template <typename T> __device__ __forceinline__ T ldsld(unsigned a) { return *(LAS T*)(size_t)a; }
template <typename T> __device__ __forceinline__ void ldsst(unsigned a, T v) { *(LAS T*)(size_t)a = v; }
#define SB() __builtin_amdgcn_sched_barrier(0)
__device__ __forceinline__ void barx() { __builtin_amdgcn_s_waitcnt(0xC07F); __builtin_amdgcn_s_barrier(); SB(); }
#define VMWAIT(n) asm volatile("s_waitcnt vmcnt(" #n ")" ::: "memory")
__device__ __forceinline__ rsrc_t mkrs(const void* p, size_t bytes) { return __builtin_amdgcn_make_buffer_rsrc((void*)p, 0, (unsigned)bytes, 0x00020000); }
__device__ __forceinline__ v4u ld16(rsrc_t r, unsigned vo, unsigned so) { return __builtin_amdgcn_raw_buffer_load_b128(r, vo, so, 16); }
__device__ __forceinline__ v2u ld8(rsrc_t r, unsigned vo, unsigned so) { return __builtin_amdgcn_raw_buffer_load_b64(r, vo, so, 16); }
__device__ __forceinline__ unsigned ld4(rsrc_t r, unsigned vo, unsigned so) { return __builtin_amdgcn_raw_buffer_load_b32(r, vo, so, 16); }
__device__ __forceinline__ void st16(v4u v, rsrc_t r, unsigned vo, unsigned so) {
  __builtin_amdgcn_raw_buffer_store_b128(v, r, vo, so, 16);
  asm volatile("s_nop 1" :: "v"(v));
}
__device__ __forceinline__ void st8(v2u v, rsrc_t r, unsigned vo, unsigned so) { __builtin_amdgcn_raw_buffer_store_b64(v, r, vo, so, 16); }
__device__ __forceinline__ void st4(unsigned v, rsrc_t r, unsigned vo, unsigned so) { __builtin_amdgcn_raw_buffer_store_b32(v, r, vo, so, 16); }
__device__ __forceinline__ f32x4 smf(v4u a, v8u b, f32x4 c, int idx) {
  return __builtin_amdgcn_smfmac_f32_16x16x64_bf16(__builtin_bit_cast(bf16x8, a), __builtin_bit_cast(bf16x16, b), c, idx, 0, 0);
}
__device__ __forceinline__ f32x4 mf32(v4u a, v4u b, f32x4 c) {
  return __builtin_amdgcn_mfma_f32_16x16x32_bf16(__builtin_bit_cast(bf16x8, a), __builtin_bit_cast(bf16x8, b), c, 0, 0, 0);
}
__device__ __forceinline__ v2u trrd(unsigned a) { return __builtin_bit_cast(v2u, __builtin_amdgcn_ds_read_tr16_b64_v4bf16((LAS bf16x4*)(size_t)a)); }
__device__ __forceinline__ v2u pack4(f32x4 v) { return (v2u){cvtpk(v[0], v[1]), cvtpk(v[2], v[3])}; }
__device__ __forceinline__ v4u cat(v2u a, v2u b) { return (v4u){a.x, a.y, b.x, b.y}; }
__device__ __forceinline__ v8u cat8(v4u a, v4u b) { return __builtin_shufflevector(a, b, 0, 1, 2, 3, 4, 5, 6, 7); }
__device__ __forceinline__ void dma16(const void* gsrc, unsigned lds_dst) {
  unsigned keep;
  asm volatile("s_mov_b32 %0, m0\n\ts_mov_b32 m0, %2\n\ts_nop 0\n\tglobal_load_lds_dwordx4 %1, off sc1\n\ts_mov_b32 m0, %0"
               : "=&s"(keep) : "v"(gsrc), "s"(lds_dst) : "memory");
}
__device__ __forceinline__ void dma16x4(const void* gsrc, unsigned lds_dst) {
  unsigned keep;
  asm volatile("s_mov_b32 %0, m0\n\ts_mov_b32 m0, %2\n\ts_nop 0\n\tglobal_load_lds_dwordx4 %1, off sc1\n\tglobal_load_lds_dwordx4 %1, off offset:1024 sc1\n\t"
               "global_load_lds_dwordx4 %1, off offset:2048 sc1\n\tglobal_load_lds_dwordx4 %1, off offset:3072 sc1\n\ts_mov_b32 m0, %0"
               : "=&s"(keep) : "v"(gsrc), "s"(lds_dst) : "memory");
}
__device__ __forceinline__ void dma16x2(const void* gsrc, unsigned lds_dst) {
  unsigned keep;
  asm volatile("s_mov_b32 %0, m0\n\ts_mov_b32 m0, %2\n\ts_nop 0\n\tglobal_load_lds_dwordx4 %1, off sc1\n\tglobal_load_lds_dwordx4 %1, off offset:1024 sc1\n\t"
               "s_mov_b32 m0, %0" : "=&s"(keep) : "v"(gsrc), "s"(lds_dst) : "memory");
}
__device__ __forceinline__ void dma16p(const void* gsrc, unsigned lds_dst) {
  unsigned keep;
  asm volatile("s_mov_b32 %0, m0\n\ts_mov_b32 m0, %2\n\ts_nop 0\n\tglobal_load_lds_dwordx4 %1, off\n\ts_mov_b32 m0, %0"
               : "=&s"(keep) : "v"(gsrc), "s"(lds_dst) : "memory");
}
__device__ __forceinline__ unsigned xcc_id() { return (unsigned)__builtin_amdgcn_s_getreg(20 | (3 << 11)) & 0xfu; }
__device__ __forceinline__ unsigned umax4(v4u v) { return max(max(v.x, v.y), max(v.z, v.w)); }
__device__ __forceinline__ unsigned rt32() { return (unsigned)__builtin_amdgcn_s_memrealtime(); }
__device__ __forceinline__ unsigned cy32() { return (unsigned)__builtin_amdgcn_s_memtime(); }
__device__ __forceinline__ void wait_cy(unsigned c) { while ((int)(cy32() - c) < 0) {} }
template <int CTRL> __device__ __forceinline__ float dppf(float v) {
  return __builtin_bit_cast(float, __builtin_amdgcn_update_dpp(0, __builtin_bit_cast(int, v), CTRL, 0xf, 0xf, true));
}
__device__ __forceinline__ float row16_sum(float x) { x += dppf<0xB1>(x); x += dppf<0x4E>(x); x += dppf<0x141>(x); x += dppf<0x140>(x); return x; }
__device__ __forceinline__ float xq_max(float x) {
  auto s = __builtin_amdgcn_permlane16_swap(__float_as_uint(x), __float_as_uint(x), false, false);
  x = fmaxf(__uint_as_float(s[0]), __uint_as_float(s[1]));
  auto t = __builtin_amdgcn_permlane32_swap(__float_as_uint(x), __float_as_uint(x), false, false);
  return fmaxf(__uint_as_float(t[0]), __uint_as_float(t[1]));
}
__device__ __forceinline__ float xq_sum(float x) {
  auto s = __builtin_amdgcn_permlane16_swap(__float_as_uint(x), __float_as_uint(x), false, false);
  x = __uint_as_float(s[0]) + __uint_as_float(s[1]);
  auto t = __builtin_amdgcn_permlane32_swap(__float_as_uint(x), __float_as_uint(x), false, false);
  return __uint_as_float(t[0]) + __uint_as_float(t[1]);
}
__host__ __device__ constexpr int smf_col(int qb, int j) {
  return 32 * (j >> 3) + 16 * (qb >> 1) + 8 * (((j & 7) >> 1) & 1) + 4 * (qb & 1) + 2 * ((j & 7) >> 2) + (j & 1);
}

#define STAMP(slot_, i_) do {} while (0)
#define RD(role_, s_, i_, v_) do {} while (0)
#define RCY(x) do {} while (0)
struct Ctx { rsrc_t rc; gu32* abortw; bool dead; unsigned slot; int seg; };
__device__ __forceinline__ bool flags_ready(const Ctx& c, unsigned vo, unsigned need) {
  const unsigned v = ld4(c.rc, vo, 0);
  asm volatile("" ::: "memory");
  return !__any(v < need);
}
__device__ __forceinline__ void flags_wait(Ctx& c, unsigned vo, unsigned need) {
#pragma unroll 1
  for (unsigned spins = 0; !c.dead; ++spins) {
    if (flags_ready(c, vo, need)) return;
    __builtin_amdgcn_s_sleep(4);
    if ((spins & 63u) == 63u) {
      if (spins > 40000u) __hip_atomic_store(c.abortw, 1u, RLX_AGENT);
      if (__builtin_amdgcn_readfirstlane(__hip_atomic_load(c.abortw, RLX_AGENT))) c.dead = true;
    }
  }
}
template <int BASE>
__device__ __forceinline__ void seg_wait(Ctx& c, int s, int& known, int lane) {
#pragma unroll 1
  for (unsigned spins = 0; known <= s && !c.dead; ++spins) {
    const unsigned v = ld4(c.rc, (BASE + lane * 4) * 4, 0);
    asm volatile("" ::: "memory");
    const unsigned long long miss = __ballot(v == 0u);
    known = miss ? __builtin_ctzll(miss) : 64;
    if (known > s) return;
    __builtin_amdgcn_s_sleep(4);
    if ((spins & 63u) == 63u) {
      if (spins > 40000u) __hip_atomic_store(c.abortw, 1u, RLX_AGENT);
      if (__builtin_amdgcn_readfirstlane(__hip_atomic_load(c.abortw, RLX_AGENT))) c.dead = true;
    }
  }
}
__device__ __forceinline__ unsigned ld4_async(const void* p) {
  unsigned v;
  asm volatile("global_load_dword %0, %1, off sc1" : "=v"(v) : "v"(p) : "memory");
  return v;
}
#define PIN(x) asm volatile("" : "+v"(x))
__device__ __forceinline__ void flag_set(const Ctx& c, unsigned word, unsigned val) { st4(val, c.rc, word * 4u, 0); }

constexpr int PRE_BT = 256, PRE_MOD = 60, PRE_CAL = 16, PRE_GRID = PRE_BT + PRE_MOD + PRE_CAL;

__global__ void __launch_bounds__(256, 1) k_pre(Args a) {
  __shared__ __attribute__((aligned(16))) char sm[32768];
  const unsigned L0 = (unsigned)(size_t)(LAS char*)sm;
  const int tid = threadIdx.x, lane = tid & 63, w = __builtin_amdgcn_readfirstlane(tid >> 6), b = blockIdx.x;
  if (b < PRE_BT) {
    const int h = b >> 3, part = b & 7;
    for (int i = tid; i < 192 * 10; i += 256) {
      const int e = i / 10, ch = i % 10;
      ldsst<v4u>(L0 + e * 160 + ch * 16, *(const v4u*)(a.Wo + ((size_t)h * DH + e) * DM + part * 80 + ch * 8));
    }
    for (int i = b * 256 + tid; i < NSEG * 4096; i += PRE_BT * 256) {
      const unsigned s = (unsigned)i >> 12, byte = ((unsigned)i & 4095u) * 16;
      v4u v = {0xFFFFFFFFu, 0xFFFFFFFFu, 0xFFFFFFFFu, 0xFFFFFFFFu};
      const unsigned o = ((byte >> 14) << 13) + (byte & 8191u);
      if (s == 0 && o < 10240) {
        v = *(const v4u*)(a.mem0 + ((o >> 4) & 7) * DM + (o >> 7) * 8);
        for (int k = 0; k < 4; ++k) v[k] = min(v[k], 0xFFFFFFFEu);
      }
      *(v4u*)(a.ws + O_XR + (size_t)s * XSTEP + byte) = v;
    }
    if (b * 256 + tid < C_WORDS) ((unsigned*)(a.ws + O_CTL))[b * 256 + tid] = 0u;
    __syncthreads();
    v4u bw[6][5];
#pragma unroll
    for (int ks = 0; ks < 6; ++ks)
#pragma unroll
      for (int nt = 0; nt < 5; ++nt) {
        const unsigned ad = L0 + (32 * ks + 8 * (lane >> 4) + ((lane & 15) >> 2)) * 160 + nt * 32 + (lane & 3) * 8;
        bw[ks][nt] = cat(trrd(ad), trrd(ad + 4 * 160));
      }
    for (int T = w; T < 10; T += 4) {
      f32x4 acc[4][5];
#pragma unroll
      for (int tp = 0; tp < 4; ++tp) {
        const int k = 64 * T + smf_col((lane & 15) >> 2, 4 * tp + (lane & 3));
        const u16* ap = a.Wv + (size_t)k * HD + h * DH + 8 * (lane >> 4);
        v4u af[6];
#pragma unroll
        for (int ks = 0; ks < 6; ++ks) af[ks] = *(const v4u*)(ap + 32 * ks);
#pragma unroll
        for (int nt = 0; nt < 5; ++nt) {
          acc[tp][nt] = (f32x4){0.f, 0.f, 0.f, 0.f};
#pragma unroll
          for (int ks = 0; ks < 6; ++ks) acc[tp][nt] = mf32(af[ks], bw[ks][nt], acc[tp][nt]);
        }
      }
#pragma unroll
      for (int nt = 0; nt < 5; ++nt) {
        const int cg = part * 10 + 2 * nt + ((lane & 15) >> 3);
        char* dst = a.ws + O_BT + ((size_t)((cg * 4 + (h >> 3)) * 40 + T * 4 + ((h >> 1) & 3))) * 2048 + ((lane >> 4) * 16 + (h & 1) * 8 + (lane & 7)) * 32;
        *(v4u*)dst = cat(pack4(acc[0][nt]), pack4(acc[1][nt]));
        *(v4u*)(dst + 16) = cat(pack4(acc[2][nt]), pack4(acc[3][nt]));
      }
    }
  } else if (b < PRE_BT + PRE_MOD) {
    const int n0 = (b - PRE_BT) * 64 + (tid & 7) * 8, k0 = (tid >> 3) * 20;
    float acc[8] = {0.f, 0.f, 0.f, 0.f, 0.f, 0.f, 0.f, 0.f};
    v4u wv[20];
#pragma unroll
    for (int k = 0; k < 20; ++k) wv[k] = *(const v4u*)(a.wmod + (size_t)(k0 + k) * (6 * DM) + n0);
#pragma unroll
    for (int k = 0; k < 20; ++k) {
      const float cv = bf2f(a.c[k0 + k]);
      const float sc = rbf(cv / (1.f + __expf(-cv)));
#pragma unroll
      for (int j = 0; j < 4; ++j) { acc[2 * j] += sc * blo(wv[k][j]); acc[2 * j + 1] += sc * bhi(wv[k][j]); }
    }
#pragma unroll
    for (int j = 0; j < 8; ++j) ldsst<float>(L0 + ((tid >> 3) * 64 + (tid & 7) * 8 + j) * 4, acc[j]);
    __syncthreads();
    if (tid < 64) {
      float s = 0.f;
      for (int k = 0; k < 32; ++k) s += ldsld<float>(L0 + (k * 64 + tid) * 4);
      const int n = (b - PRE_BT) * 64 + tid, which = n / DM;
      const float v = rbf(s);
      ((float*)(a.ws + O_MOD))[n] = v;
      if (which != 2 && which != 5) {
        const bool isc = which == 1 || which == 4;
        ((u16*)(a.ws + O_MODP))[((which >= 3 ? 2 : 0) + (isc ? 0 : 1)) * DM + n % DM] = (u16)(cvtpk(isc ? 1.f + v : v, 0.f) & 0xFFFFu);
      }
    }
  } else {
    const int s = (b - PRE_BT - PRE_MOD) * 4 + w;
    const rsrc_t r = mkrs(a.ws + O_XR, (size_t)NSEG * XSTEP);
    unsigned best[2] = {0xFFFFFFFFu, 0xFFFFFFFFu};
    for (int p = 0; p < 4; ++p)
      for (int hf = 0; hf < 2; ++hf) {
        const unsigned off = s * XSTEP + 65536u + hf * 8192u + p * 2048u;
        st4(0u, r, off, 0);
        VMWAIT(0);
        const unsigned t0 = cy32();
        asm volatile("s_waitcnt lgkmcnt(0)" ::: "memory");
        const unsigned v = ld4(r, off, 0);
        asm volatile("s_waitcnt vmcnt(0)" :: "v"(v) : "memory");
        best[hf] = min(best[hf], cy32() - t0);
      }
    unsigned nearh = best[1] < best[0] ? 1u : 0u;
    if (xcc_id() >= 4) nearh ^= 1u;
    if (lane == 0) ((unsigned*)(a.ws + O_SEL))[s] = nearh;
  }
}

template <bool COH>
__device__ __forceinline__ void ln_segment(const char* src, const char* gs, rsrc_t rdst, unsigned dbase, int w, int lane) {
  const rsrc_t rs = mkrs(src, 32 * DM * 2);
  const int c16 = lane & 15, row0 = w * 8 + (lane >> 4);
  v4u v[2][5], g[5], sh[5];
#pragma unroll
  for (int p = 0; p < 2; ++p)
#pragma unroll
    for (int i = 0; i < 5; ++i) v[p][i] = __builtin_amdgcn_raw_buffer_load_b128(rs, (row0 + 4 * p) * (DM * 2) + (c16 + 16 * i) * 16, 0, COH ? 16 : 0);
#pragma unroll
  for (int i = 0; i < 5; ++i) { g[i] = *(const v4u*)(gs + (c16 + 16 * i) * 16); sh[i] = *(const v4u*)(gs + DM * 2 + (c16 + 16 * i) * 16); }
#pragma unroll
  for (int p = 0; p < 2; ++p) {
    f32x2 f[20];
    f32x2 s2 = {0.f, 0.f};
#pragma unroll
    for (int i = 0; i < 20; ++i) { f[i] = (f32x2){blo(v[p][i >> 2][i & 3]), bhi(v[p][i >> 2][i & 3])}; s2 += f[i]; }
    const float mean = row16_sum(s2.x + s2.y) * (1.f / DM);
    f32x2 q2 = {0.f, 0.f};
#pragma unroll
    for (int i = 0; i < 20; ++i) { const f32x2 d = f[i] - mean; q2 += d * d; }
    const float m = rbf(mean), var = rbf(row16_sum(q2.x + q2.y) * (1.f / DM));
    const float inv = 1.f / rbf(sqrtf(rbf(var + 1e-5f)));
    const int row = row0 + 4 * p;
#pragma unroll
    for (int i = 0; i < 5; ++i) {
      v4u o;
#pragma unroll
      for (int j = 0; j < 4; ++j) {
        const f32x2 dd = f[4 * i + j] - m;
        const unsigned d = cvtpk(dd.x, dd.y);
        const f32x2 nn = (f32x2){blo(d), bhi(d)} * inv;
        const unsigned n = cvtpk(nn.x, nn.y);
        const f32x2 tt = (f32x2){blo(n), bhi(n)} * (f32x2){blo(g[i][j]), bhi(g[i][j])};
        const unsigned t = cvtpk(tt.x, tt.y);
        const f32x2 oo = (f32x2){blo(t), bhi(t)} + (f32x2){blo(sh[i][j]), bhi(sh[i][j])};
        o[j] = cvtpk(oo.x, oo.y);
      }
      const int ch = c16 + 16 * i;
      st16(o, rdst, ((row >> 4) * 20 + (ch >> 2)) * 1024 + ((ch & 3) * 16 + (row & 15)) * 16, dbase);
    }
  }
}

struct Sched {
  unsigned g1, g2, e1, e2; int r1, r2, w1, w2, c1, c2; bool v1, v2;
  __device__ __forceinline__ void init(int W1, int W2) { g1 = g2 = e1 = e2 = 0; r1 = r2 = -1; w1 = W1; w2 = W2; c1 = c2 = 0; v1 = v2 = false; }
  static __device__ __forceinline__ int upd(int r, int x, int c) {
    x = min(max(x, 4), 1200);
    const int f = x > r ? min(x, r + 24) : (r + x) >> 1;
    return c == 0 ? r : (r < 0 ? x : (c == 2 ? f : r));
  }
  __device__ __forceinline__ void got_b(unsigned b) {
    const bool ok = v2 && (int)(g2 - b) >= w2 - 12;
    e2 = ok ? g2 : b + w2 + 48; c2 = ok ? 2 : 1;
    r1 = upd(r1, (int)(b - e1), c1);
  }
  __device__ __forceinline__ void got_a(unsigned a) {
    const bool ok = v1 && (int)(g1 - a) >= w1 - 12;
    e1 = ok ? g1 : a + w1 + 48; c1 = ok ? 2 : 1;
    r2 = upd(r2, (int)(a - e2), c2);
    v2 = r1 >= 0; g2 = e1 + r1 + w2;
    v1 = v2 && r2 >= 0; g1 = g2 + r2 + w1;
  }
};
struct Clock {
  unsigned rt0, cy0, rte, cye; float upc, cpu;
  static __device__ __forceinline__ void edge(unsigned& r, unsigned& c) {
    const unsigned r0 = rt32();
#pragma unroll 1
    for (int k = 0; k < 64; ++k) { r = rt32(); c = cy32(); if (r != r0) break; }
  }
  __device__ __forceinline__ void init() { edge(rt0, cy0); rte = rt0; cye = cy0; cpu = 5.9f; upc = 1.f / 5.9f; }
  __device__ __forceinline__ void sync() {
    edge(rte, cye);
    const float dr = (float)(rte - rt0) * 4.f, dc = (float)(cye - cy0);
    cpu = __builtin_bit_cast(float, __builtin_amdgcn_readfirstlane(__builtin_bit_cast(int, dc * __builtin_amdgcn_rcpf(dr))));
    upc = __builtin_bit_cast(float, __builtin_amdgcn_readfirstlane(__builtin_bit_cast(int, dr * __builtin_amdgcn_rcpf(dc))));
  }
  __device__ __forceinline__ unsigned time_of(unsigned c) const { return rte * 4u + (unsigned)__builtin_amdgcn_readfirstlane((int)((float)(int)(c - cye) * upc)); }
  __device__ __forceinline__ unsigned cycle_of(unsigned t) const { return cye + (unsigned)__builtin_amdgcn_readfirstlane((int)((float)(int)(t - rte * 4u) * cpu)); }
};
#define DPPM " row_mask:0xf bank_mask:0xf\n\t"
__device__ __forceinline__ int wave_max_i32(int v) {
  int x, y;
  asm("s_nop 1\n\t"
      "v_max_i32_dpp %0, %2, %2 quad_perm:[1,0,3,2]" DPPM "s_nop 1\n\t"
      "v_max_i32_dpp %0, %0, %0 quad_perm:[2,3,0,1]" DPPM "s_nop 1\n\t"
      "v_max_i32_dpp %0, %0, %0 row_half_mirror" DPPM "s_nop 1\n\t"
      "v_max_i32_dpp %1, %0, %0 row_mirror" DPPM
      "v_max_i32_dpp %0, %0, %0 row_mirror" DPPM "s_nop 1\n\t"
      "v_permlane16_swap_b32 %0, %1\n\ts_nop 0\n\t"
      "v_max_i32 %0, %0, %1\n\t"
      "v_mov_b32 %1, %0\n\ts_nop 1\n\t"
      "v_permlane32_swap_b32 %0, %1\n\ts_nop 0\n\t"
      "v_max_i32 %0, %0, %1\n\ts_nop 0" : "=&v"(x), "=&v"(y) : "v"(v));
  return __builtin_amdgcn_readfirstlane(x);
}
__device__ __forceinline__ float red32_max(float a, float b) {
  float x, y;
  asm("v_max_f32 %0, %2, %3\n\ts_nop 1\n\t"
      "v_max_f32_dpp %0, %0, %0 quad_perm:[1,0,3,2]" DPPM "s_nop 1\n\t"
      "v_max_f32_dpp %0, %0, %0 quad_perm:[2,3,0,1]" DPPM "s_nop 1\n\t"
      "v_max_f32_dpp %0, %0, %0 row_half_mirror" DPPM "s_nop 1\n\t"
      "v_max_f32_dpp %1, %0, %0 row_mirror" DPPM
      "v_max_f32_dpp %0, %0, %0 row_mirror" DPPM "s_nop 1\n\t"
      "v_permlane16_swap_b32 %0, %1\n\ts_nop 0\n\t"
      "v_max_f32 %0, %0, %1" : "=&v"(x), "=&v"(y) : "v"(a), "v"(b));
  return x;
}
__device__ __forceinline__ float red32_sum(float a, float b) {
  float x, y;
  asm("v_add_f32 %0, %2, %3\n\ts_nop 1\n\t"
      "v_add_f32_dpp %0, %0, %0 quad_perm:[1,0,3,2]" DPPM "s_nop 1\n\t"
      "v_add_f32_dpp %0, %0, %0 quad_perm:[2,3,0,1]" DPPM "s_nop 1\n\t"
      "v_add_f32_dpp %0, %0, %0 row_half_mirror" DPPM "s_nop 1\n\t"
      "v_add_f32_dpp %1, %0, %0 row_mirror" DPPM
      "v_add_f32_dpp %0, %0, %0 row_mirror" DPPM "s_nop 1\n\t"
      "v_permlane16_swap_b32 %0, %1\n\ts_nop 0\n\t"
      "v_add_f32 %0, %0, %1" : "=&v"(x), "=&v"(y) : "v"(a), "v"(b));
  return x;
}
struct Stamps { unsigned t0, t1; };
__device__ __forceinline__ Stamps stamps_load(rsrc_t rx, unsigned o0, unsigned o1, unsigned sbase) { return Stamps{ld4(rx, o0, sbase), ld4(rx, o1, sbase)}; }
__device__ __forceinline__ unsigned stamps_latest(Stamps s, rsrc_t rx, unsigned o0, unsigned o1, unsigned sbase, unsigned ref, bool dead) {
#pragma unroll 1
  for (int k = 0; k < 4000; ++k) {
    if (!__any(s.t0 == 0xFFFFFFFFu || s.t1 == 0xFFFFFFFFu) || dead) break;
    __builtin_amdgcn_s_sleep(1);
    asm volatile("" ::: "memory");
    s = stamps_load(rx, o0, o1, sbase);
  }
  return ref + (unsigned)wave_max_i32(max((int)(s.t0 - ref), (int)(s.t1 - ref)));
}
template <int NB>
__device__ __forceinline__ void poll(rsrc_t r, const unsigned (&voff)[NB], unsigned soff, v4u (&v)[NB], Ctx& c) {
#pragma unroll 1
  for (unsigned spins = 0;; ++spins) {
#pragma unroll
    for (int k = 0; k < NB; ++k) v[k] = ld16(r, voff[k], soff);
    unsigned m = 0;
#pragma unroll
    for (int k = 0; k < NB; ++k) m = max(m, umax4(v[k]));
    asm volatile("" ::: "memory");
    if (!__any(m == 0xFFFFFFFFu) || c.dead) return;
    if ((spins & 255u) == 255u) {
      if (spins > 60000u) __hip_atomic_store(c.abortw, 1u, RLX_AGENT);
      if (__builtin_amdgcn_readfirstlane(__hip_atomic_load(c.abortw, RLX_AGENT))) c.dead = true;
    }
  }
}
__device__ __forceinline__ v2u fold_pack(f32x4 a, f32x4 b) {
  float f[4];
#pragma unroll
  for (int r = 0; r < 4; ++r) {
    auto sw = __builtin_amdgcn_permlane32_swap(__float_as_uint(a[r]), __float_as_uint(b[r]), false, false);
    f[r] = __uint_as_float(sw[0]) + __uint_as_float(sw[1]);
  }
  return (v2u){cvtpk(f[0], f[1]), cvtpk(f[2], f[3])};
}

__device__ __forceinline__ void stage384(const char* W, size_t ldb, int row0, int n3, unsigned dst, int w, int lane) {
#pragma unroll 1
  for (int i = w; i < 3 * n3; i += 4) {
    const unsigned byte = (i % 3) * 1024 + lane * 16, row = byte / 384, col = byte % 384;
    dma16p(W + (size_t)(row0 + 8 * (i / 3) + row) * ldb + col, dst + i * 1024);
  }
}

constexpr int L1_IMG = 0, L1_PART = 10240, L1_W = L1_PART + 6144, MAIN_LDS = L1_W + 4 * 18 * 2048;
static_assert(MAIN_LDS == 163840, "lds");

template <int W> __device__ constexpr int l1_slot(int T) { return T >= 8 ? T : ((T - W) & 3) == 0 ? (T >> 2) : (T < 4 ? 1 : 4) + ((T - W) & 3); }
template <int W>
__device__ __forceinline__ void l1_weights(const Args& a, const int h, const unsigned L0, const int lane, v8u (&wq)[3][10], v8u (&wk)[3][4]) {
  const unsigned wl = L0 + L1_W + W * 18 * 2048 + lane * 16;
  const unsigned trb = (16 * (lane >> 5) + 4 * ((lane >> 4) & 1) + ((lane & 15) >> 2)) * 384 + (3 * W) * 32 + (lane & 3) * 8;
  auto frag = [&](unsigned base, int t) {
    const unsigned ad = base + trb + t * 32;
    const v2u r0 = trrd(ad), r1 = trrd(ad + 8 * 384), r2 = trrd(ad + 32 * 384), r3 = trrd(ad + 40 * 384);
    return (v8u){r0.x, r1.x, r0.y, r1.y, r2.x, r3.x, r2.y, r3.y};
  };
  auto stage = [&](int r) { stage384((const char*)((r < 5 ? a.Wq : a.Wk) + h * DH), HD * 2, 128 * (r % 5), 16, L0 + (r & 1) * 49152, W, lane); };
  v8u tmp[6][3];
  stage(0); stage(1);
#pragma unroll
  for (int r = 0; r < 10; ++r) {
    if (r < 9) VMWAIT(12); else VMWAIT(0);
    barx();
#pragma unroll
    for (int k = 0; k < 2; ++k)
#pragma unroll
      for (int t = 0; t < 3; ++t) {
        const int T = 2 * (r % 5) + k, sl = l1_slot<W>(T);
        const v8u f = frag(L0 + (r & 1) * 49152 + k * 24576, t);
        if (r < 5) wq[t][sl] = f; else if (T < 4) wk[t][sl == 0 ? 0 : (sl - 1) & 3] = f; else tmp[T < 4 ? 0 : T - 4][t] = f;
      }
    barx();
    if (r < 8) stage(r + 2);
  }
#pragma unroll
  for (int T = 4; T < 10; ++T)
#pragma unroll
    for (int t = 0; t < 3; ++t) {
      const int sl = l1_slot<W>(T), n = sl == 1 ? t : 3 + t * 5 + (sl - 5);
      const v8u f = tmp[T - 4][t];
      ldsst<v4u>(wl + n * 2048, __builtin_shufflevector(f, f, 0, 1, 2, 3));
      ldsst<v4u>(wl + n * 2048 + 1024, __builtin_shufflevector(f, f, 4, 5, 6, 7));
    }
}

__device__ __forceinline__ void layer1(const Args& a, const int h, const unsigned L0, Ctx& cx) {
  const int tid = threadIdx.x, lane = tid & 63, w = __builtin_amdgcn_readfirstlane(tid >> 6);
  const rsrc_t rx = mkrs(a.ws + O_XR, (size_t)NSEG * XSTEP), rk = mkrs(a.ws + O_KX, (size_t)S * HD * 2), rkm = mkrs(a.ws + O_KM, (size_t)NSEG * H * 6144);
  const unsigned wl = L0 + L1_W + w * 18 * 2048 + lane * 16;
  v8u wq[3][10], wk[3][4];
  if (w == 0) l1_weights<0>(a, h, L0, lane, wq, wk); else if (w == 1) l1_weights<1>(a, h, L0, lane, wq, wk);
  else if (w == 2) l1_weights<2>(a, h, L0, lane, wq, wk); else l1_weights<3>(a, h, L0, lane, wq, wk);
  const unsigned long long selmask = __ballot(((const unsigned*)(a.ws + O_SEL))[lane] != 0u);
  const unsigned sel_lo = __builtin_amdgcn_readfirstlane((unsigned)selmask), sel_hi = __builtin_amdgcn_readfirstlane((unsigned)(selmask >> 32));
  const int sidx = (lane & 8) ? 0xEEEE : 0x4444;
  unsigned moff[3];
#pragma unroll
  for (int k = 0; k < 3; ++k) { int b = w + 4 * k; if (b >= 10) b = w; moff[k] = xmap(b * 1024 + lane * 16); }
  const unsigned img = L0 + L1_IMG + lane * 16, mst2 = img + (w < 2 ? w + 8 : w) * 1024;
  unsigned aad[3];
#pragma unroll
  for (int i = 0; i < 3; ++i) aad[i] = img + ((w + 1 + i) & 3) * 1024;
  const int qi = 2 * w + (lane >> 5), j = lane & 31;
  const unsigned poff = xmap(XP + (w >> 1) * 10240 + (w & 1) * 128 + (lane >> 5) * 16 +
                             ((j & 1) ? (8 + (h >> 4)) * 1024 + ((h >> 2) & 3) * 256 + (h & 3) * 32 + (j - 1) * 2
                                      : (h >> 2) * 1024 + (h & 3) * 256 + ((j & 15) >> 2) * 32 + ((j >> 4) * 4 + (j & 3)) * 2));
  const bool pstore = !(j & 1) || j < 8;
  const unsigned t1o = xmap(XT1 + (lane & 31) * 16), t2o0 = xmap(XT2 + lane * 16), t2o1 = xmap(XT2 + 1024 + (lane & 15) * 16);
  const int dq = (lane & 15) - 4 * (lane >> 4);
  const v4u ident = {dq == 0 ? 0x3F80u : dq == 1 ? 0x3F800000u : 0u, dq == 2 ? 0x3F80u : dq == 3 ? 0x3F800000u : 0u, 0u, 0u};
  const unsigned kxo = (h * 4 + w) * 3072 + lane * 16, kfo = (C_KF + h * 32) * 4;
  STAMP(cx.slot, 1);
  v4u kx[3];
  flags_wait(cx, kfo, 1u);
#pragma unroll
  for (int t = 0; t < 3; ++t) kx[t] = ld16(rk, kxo + t * 1024, 0);
  unsigned kready = __builtin_amdgcn_readfirstlane(ld4(cx.rc, kfo, 0));
  Sched sc; sc.init(a.w1q, a.w2q);
  Clock ck; ck.init();
  unsigned gate = cy32(), pbase = 0;
  const f32x4 Z4 = {0.f, 0.f, 0.f, 0.f};
#pragma unroll 1
  for (int s = 0; s < NSEG; ++s) {
    const unsigned sbase = s * XSTEP + (((s < 32 ? sel_lo : sel_hi) >> (s & 31)) & 1u) * 8192u;
    f32x4 aq[3], ak[3];
#pragma unroll
    for (int t = 0; t < 3; ++t) { aq[t] = Z4; ak[t] = Z4; asm volatile("" : "+v"(aq[t]), "+v"(ak[t])); }
    v4u llo[3], lhi[3];
#pragma unroll
    for (int i = 0; i < 3; ++i) { llo[i] = ldsld<v4u>(wl + i * 2048); lhi[i] = ldsld<v4u>(wl + i * 2048 + 1024); asm volatile("" : "+v"(llo[i]), "+v"(lhi[i])); }
    SB();
    RCY(d_rdy);
    wait_cy(gate);
    v4u mv[3];
    poll<3>(rx, moff, sbase, mv, cx);
    RCY(d_det);
    ldsst<v4u>(img + w * 1024, mv[0]);
    ldsst<v4u>(img + (w + 4) * 1024, mv[1]);
    ldsst<v4u>(mst2, mv[2]);
    SB();
#pragma unroll
    for (int t = 0; t < 3; ++t) { aq[t] = smf(mv[0], wq[t][0], aq[t], sidx); ak[t] = smf(mv[0], wk[t][0], ak[t], sidx); }
    SB();
    barx();
    v4u am[8];
#pragma unroll
    for (int i = 0; i < 8; ++i) am[i] = ldsld<v4u>(i < 6 ? aad[i % 3] + (i / 3) * 4096 : img + (i + 2) * 1024);
    SB();
#pragma unroll
    for (int t = 0; t < 3; ++t) {
      aq[t] = smf(mv[1], wq[t][1], aq[t], sidx);
      ak[t] = smf(mv[1], cat8(llo[t], lhi[t]), ak[t], sidx);
      SB();
    }
    if (tid == 192) flag_set(cx, C_MF1 + h * 32, 4u * s);
    SB();
    f32x4 s0 = Z4, s1 = Z4, sm = Z4, xt;
    v4u X01;
    v2u xa;
    unsigned sw[8];
    float ff[4];
#pragma unroll
    for (int t = 0; t < 3; ++t) {
#pragma unroll
      for (int i = 0; i < 8; ++i) {
        const int n = 3 + t * 5 + (i - 3);
        aq[t] = smf(am[i], wq[t][2 + i], aq[t], sidx);
        SB();
        if (t == 0 && i < 3) llo[i] = ldsld<v4u>(wl + (3 + i) * 2048);
        if (i >= 4 && n + 2 < 18) lhi[(n - 1) % 3] = ldsld<v4u>(wl + (n + 2) * 2048 + 1024);
        if (i == 0 && t > 0 && t * 5 + 5 < 18) lhi[(t * 5 + 2) % 3] = ldsld<v4u>(wl + (t * 5 + 5) * 2048 + 1024);
        if (t > 0) {
          if (i == 1) { for (int r = 0; r < 2; ++r) { auto x = __builtin_amdgcn_permlane32_swap(__float_as_uint(aq[t - 1][r]), __float_as_uint(ak[t - 1][r]), false, false); sw[2 * r] = x[0]; sw[2 * r + 1] = x[1]; }
                        ff[0] = __uint_as_float(sw[0]) + __uint_as_float(sw[1]); }
          if (i == 2) { ff[2] = __uint_as_float(sw[4]) + __uint_as_float(sw[5]); ff[3] = __uint_as_float(sw[6]) + __uint_as_float(sw[7]); }
          if (i == 3) xt = mf32((v4u){xa.x, xa.y, 0u, 0u}, ident, Z4);
          if (i == 5) X01[2 * (t - 1) + 1] = cvtpk(xt[2], xt[3]);
          if (t == 2 && i == 6) s0 = mf32(kx[0], X01, s0);
          if (t == 2 && i == 7) sm = mf32(X01, X01, sm);
        }
        SB();
        ak[t] = smf(am[i], i < 3 ? wk[t][i < 3 ? 1 + i : 0] : cat8(llo[(n + 18) % 3], lhi[(n + 18) % 3]), ak[t], sidx);
        SB();
        if (t == 0 && i < 3) lhi[i] = ldsld<v4u>(wl + (3 + i) * 2048 + 1024);
        if (i >= 3 && n + 3 < 18) llo[n % 3] = ldsld<v4u>(wl + (n + 3) * 2048);
        if (t > 0) {
          if (i == 1) { for (int r = 2; r < 4; ++r) { auto x = __builtin_amdgcn_permlane32_swap(__float_as_uint(aq[t - 1][r]), __float_as_uint(ak[t - 1][r]), false, false); sw[2 * r] = x[0]; sw[2 * r + 1] = x[1]; }
                        ff[1] = __uint_as_float(sw[2]) + __uint_as_float(sw[3]); }
          if (i == 2) { xa.x = cvtpk(ff[0], ff[1]); xa.y = cvtpk(ff[2], ff[3]); }
          if (i == 4) X01[2 * (t - 1)] = cvtpk(xt[0], xt[1]);
          if (t == 2 && i == 6) s1 = mf32(kx[1], X01, s1);
        }
        SB();
      }
    }
    xa = fold_pack(aq[2], ak[2]);
    xt = mf32((v4u){xa.x, xa.y, 0u, 0u}, ident, Z4);
    const v2u X2 = pack4(xt);
    unsigned cy_pub = 0, t_pub = 0;
    if (s < NSEG - 1) {
      const v4u xz = {X2.x, X2.y, 0u, 0u};
      s0 = mf32(kx[2], xz, s0);
      s1 = mf32(kx[2], (v4u){0u, 0u, X2.x, X2.y}, s1);
      sm = mf32(xz, xz, sm);
      if ((lane & 8) == 0) {
        const unsigned pa = L0 + L1_PART + w * 1536 + (lane & 7) * 64 + (lane >> 4) * 16;
        ldsst<f32x4>(pa, s0); ldsst<f32x4>(pa + 512, s1); ldsst<f32x4>(pa + 1024, sm);
      }
      barx();
      const unsigned ra = L0 + L1_PART + (j >> 4) * 512 + qi * 64 + (j & 15) * 4;
      const unsigned rb = L0 + L1_PART + 1024 + qi * 64 + (8 + (j & 7)) * 4;
      const float x0 = ldsld<float>(ra), x1 = ldsld<float>(ra + 1536), x2 = ldsld<float>(ra + 3072), x3 = ldsld<float>(ra + 4608);
      const float y0 = ldsld<float>(rb), y1 = ldsld<float>(rb + 1536), y2 = ldsld<float>(rb + 3072), y3 = ldsld<float>(rb + 4608);
      SB();
      const unsigned r1 = cvtpk((x0 + x1) + (x2 + x3), (y0 + y1) + (y2 + y3));
      const unsigned r2 = cvtpk(blo(r1) * SCALE, bhi(r1) * SCALE);
      const float sx = blo(r2), sy = bhi(r2);
      const float mx = red32_max(sx, sy);
      const float ex = __builtin_amdgcn_exp2f((sx - mx) * LOG2E), ey = __builtin_amdgcn_exp2f((sy - mx) * LOG2E);
      const float inv = __builtin_amdgcn_rcpf(red32_sum(ex, j < 8 ? ey : 0.f));
      const float own = ((j & 1) ? ey : ex) * inv, snd = ((j & 1) ? ex : ey) * inv;
      const float oth = dppf<0xB1>(snd);
      const unsigned pv = min((j & 1) ? cvtpk(oth, own) : cvtpk(own, oth), 0xFFFFFFFEu);
      if (pstore) st4(pv, rx, poff, sbase);
      SB();
      cy_pub = cy32();
      t_pub = ck.time_of(cy_pub);
      if (tid == 0) st4(min(t_pub, 0xFFFFFFFEu), rx, xmap(XT1 + h * 16), sbase);
    }
    const Stamps st2 = stamps_load(rx, t2o0, t2o1, sbase);
    const unsigned t1v = ld4(rx, t1o, pbase), kfv = ld4(cx.rc, kfo, 0);
    const bool kpre = s < NSEG - 2 && kready >= (unsigned)s + 2u;
    if (kpre) {
#pragma unroll
      for (int t = 0; t < 3; ++t) kx[t] = ld16(rk, kxo + t * 1024, (s + 1) * (H * 12288));
    }
    st16(X01, rkm, (h * 4 + w) * 1536 + lane * 16, s * (H * 6144));
    st8(X2, rkm, (h * 4 + w) * 1536 + 1024 + lane * 8, s * (H * 6144));
    SB();
    ck.sync();
    kready = __builtin_amdgcn_readfirstlane(kfv);
    if (s < NSEG - 1) {
      if (s > 0) {
        sc.got_b(stamps_latest(Stamps{t1v, t1v}, rx, t1o, t1o, pbase, t_pub, cx.dead));
        sc.got_a(stamps_latest(st2, rx, t2o0, t2o1, sbase, t_pub, cx.dead));
      }
      gate = sc.v1 ? ck.cycle_of(sc.g1) : cy_pub;
      if (!kpre && s < NSEG - 2) {
        if (kready < (unsigned)s + 2u) flags_wait(cx, kfo, s + 2);
#pragma unroll
        for (int t = 0; t < 3; ++t) kx[t] = ld16(rk, kxo + t * 1024, (s + 1) * (H * 12288));
      }
    }
    RCY(d_end); RD(h, s, 1, d_det - d_rdy); RD(h, s, 2, cy_pub - d_det); RD(h, s, 3, sc.r1); RD(h, s, 4, sc.r2); RD(h, s, 5, d_end - cy_pub); RD(h, s, 6, gate - cy_pub);
    pbase = sbase;
  }
  VMWAIT(0); barx();
  if (tid == 192) flag_set(cx, C_MF1 + h * 32, 4u * NSEG);
}

constexpr int L2_IMG = 0, L2_UM = 10240, L2_UX = L2_UM + 4096, L2_W = L2_UX + 2 * 16384, L2_MISC = L2_W + 4 * 8 * 2048;

__device__ __forceinline__ void layer2(const Args& a, const int cg, const unsigned L0, Ctx& cx) {
  const int tid = threadIdx.x, lane = tid & 63, w = __builtin_amdgcn_readfirstlane(tid >> 6);
  const rsrc_t rx = mkrs(a.ws + O_XR, (size_t)NSEG * XSTEP), rum = mkrs(a.ws + O_UM, (size_t)NSEG * 80 * 4096);
  const unsigned wl = L0 + L2_W + w * 8 * 2048 + lane * 16;
  v8u wr[8][4];
  {
    const v4u* src = (const v4u*)(a.ws + O_BT + ((size_t)(cg * 4 + w) * 40) * 2048) + lane * 2;
#pragma unroll
    for (int f = 0; f < 40; ++f) {
      const v4u lo = src[f * 128], hi = src[f * 128 + 1];
      if (f >= 32) { ldsst<v4u>(wl + (f - 32) * 2048, lo); ldsst<v4u>(wl + (f - 32) * 2048 + 1024, hi); }
      else wr[f >> 2][f & 3] = cat8(lo, hi);
    }
  }
  const unsigned long long selmask = __ballot(((const unsigned*)(a.ws + O_SEL))[lane] != 0u);
  const unsigned sel_lo = __builtin_amdgcn_readfirstlane((unsigned)selmask), sel_hi = __builtin_amdgcn_readfirstlane((unsigned)(selmask >> 32));
  const int sidx = (lane & 8) ? 0xEEEE : 0x4444;
  unsigned moff[4], po[10];
#pragma unroll
  for (int k = 0; k < 4; ++k) { int b = w + 3 * k; if (b >= 10) b = w; moff[k] = xmap(b * 1024 + lane * 16); }
#pragma unroll
  for (int u = 0; u < 10; ++u) po[u] = xmap(XP + (w & 1) * 10240 + u * 1024 + lane * 16);
  const unsigned mst0 = L0 + L2_IMG + w * 1024 + lane * 16, mst3 = L0 + L2_IMG + (w == 0 ? 9 : w) * 1024 + lane * 16;
  const unsigned umo = L0 + L2_UM + ((w >> 1) * 2 + (lane & 1)) * 1024 + ((2 * (w & 1)) * 16 + 4 * ((lane & 7) >> 1) + 2 * (lane >> 5) + ((lane >> 3) & 1)) * 16 + ((lane >> 4) & 1) * 8;
  const unsigned ooff = xmap(cg * 128 + (4 * (w & 1) + 2 * ((lane >> 3) & 1) + (lane & 1)) * 16 + (lane >> 4) * 4);
  const unsigned t1o = xmap(XT1 + (lane & 31) * 16), t2o0 = xmap(XT2 + lane * 16), t2o1 = xmap(XT2 + 1024 + (lane & 15) * 16);
  const unsigned ufo = (C_UF + (lane & 31) * 4) * 4;
  auto ux_dma = [&](int s) {
    const char* up = a.ws + O_UX + ((size_t)s * 80 + cg) * 16384 + lane * 16;
#pragma unroll
    for (int v = 0; v < 16; ++v) dma16(up + v * 1024, L0 + L2_UX + (s & 1) * 16384 + v * 1024);
  };
  STAMP(cx.slot, 1);
  if (w == 3) { flags_wait(cx, ufo, 1u); ux_dma(0); }
  unsigned ufv = 0;
  unsigned tref = cy32();
  Sched sc; sc.init(a.w1q, a.w2q);
  Clock ck; ck.init();
  unsigned pbase = 0;
  const f32x4 Z4 = {0.f, 0.f, 0.f, 0.f};
#pragma unroll 1
  for (int s = 0; s < NSEG; ++s) {
    const unsigned sbase = s * XSTEP + (((s < 32 ? sel_lo : sel_hi) >> (s & 31)) & 1u) * 8192u;
    const unsigned nbase = (s + 1) * XSTEP + (((s + 1 < 32 ? sel_lo : sel_hi) >> ((s + 1) & 31)) & 1u) * 8192u;
    f32x4 acc[4];
#pragma unroll
    for (int t = 0; t < 4; ++t) { acc[t] = Z4; asm volatile("" : "+v"(acc[t])); }
    Stamps st1 = {0u, 0u}, st2 = {0u, 0u};
    if (w == 1 && s > 0) st1 = stamps_load(rx, t1o, t1o, pbase);
    if (w < 2 && s > 0) ck.sync();
    if (w < 3) {
      if (s > 0) wait_cy(tref + a.w1q * 6 + a.l2d);
      v4u mv[4];
      poll<4>(rx, moff, sbase, mv, cx);
      ldsst<v4u>(mst0, mv[0]); ldsst<v4u>(mst0 + 3072, mv[1]); ldsst<v4u>(mst0 + 6144, mv[2]); ldsst<v4u>(mst3, mv[3]);
    }
    barx();
    v4u am[10];
#pragma unroll
    for (int T = 0; T < 10; ++T) am[T] = ldsld<v4u>(L0 + L2_IMG + T * 1024 + lane * 16);
    if (w == 1 && s > 0) st2 = stamps_load(rx, t2o0, t2o1, sbase);
    bool early = false;
    if (w == 3) {
      VMWAIT(0);
      if (s > 0 && lane == 0) flag_set(cx, C_MF2 + cg * 4, s);
      early = s + 1 < NSEG - 1 && !__any(ufv < (unsigned)s + 2u);
      if (early) ux_dma(s + 1);
    }
    SB();
    v4u lw[16];
#pragma unroll
    for (int T = 0; T < 10; ++T)
#pragma unroll
      for (int t = 0; t < 4; ++t) {
        const int i = T * 4 + t;
        acc[t] = smf(am[T], T < 8 ? wr[T < 8 ? T : 0][t] : cat8(lw[2 * (i & 7)], lw[2 * (i & 7) + 1]), acc[t], sidx);
        SB();
        if (i >= 8 && i < 24) { lw[i - 8] = ldsld<v4u>(wl + (i - 8) * 1024); SB(); }
      }
#pragma unroll
    for (int tp = 0; tp < 2; ++tp) ldsst<v2u>(umo + tp * 256, fold_pack(acc[2 * tp], acc[2 * tp + 1]));
    if (w == 1) {
      if (s > 0) {
        const unsigned ref = ck.rte * 4u;
        sc.got_b(stamps_latest(st1, rx, t1o, t1o, pbase, ref, cx.dead));
        sc.got_a(stamps_latest(st2, rx, t2o0, t2o1, sbase, ref, cx.dead));
      }
      ldsst<v2u>(L0 + L2_MISC + 8, (v2u){ck.cycle_of(sc.g2), sc.v2 ? 1u : 0u});
    }
    pbase = sbase;
    barx();
    if (w == 3 && s + 1 < NSEG - 1 && !early) { flags_wait(cx, ufo, s + 2); ux_dma(s + 1); }
    if (s < NSEG - 1) {
      if (w < 2) {
        v4u ua[20];
        const v2u gv = ldsld<v2u>(L0 + L2_MISC + 8);
#pragma unroll
        for (int u = 0; u < 16; ++u) ua[u] = ldsld<v4u>(L0 + L2_UX + (s & 1) * 16384 + u * 1024 + lane * 16);
#pragma unroll
        for (int u = 0; u < 4; ++u) ua[16 + u] = ldsld<v4u>(L0 + L2_UM + u * 1024 + lane * 16);
#pragma unroll
        for (int u = 0; u < 20; u += 4) asm volatile("" : "+v"(ua[u]), "+v"(ua[u + 1]), "+v"(ua[u + 2]), "+v"(ua[u + 3]));
        const unsigned g2 = __builtin_amdgcn_readfirstlane(gv.x);
        SB();
        RCY(d_rdy);
        if (__builtin_amdgcn_readfirstlane(gv.y)) wait_cy(g2);
        unsigned ov;
#pragma unroll 1
        for (unsigned spins = 0;; ++spins) {
          v4u pv[10];
#pragma unroll
          for (int u = 0; u < 10; ++u) pv[u] = ld16(rx, po[u], sbase);
          SB();
          f32x4 d0 = Z4, d1 = Z4;
          unsigned m = 0;
#pragma unroll
          for (int u = 0; u < 10; ++u) {
            d0 = mf32(ua[2 * u], pv[u], d0);
            d1 = mf32(ua[2 * u + 1], pv[u], d1);
            m = max(m, umax4(pv[u]));
            SB();
          }
          ov = min(cvtpk(((d0[0] + dppf<0x102>(d0[1])) + dppf<0x104>(d0[2])) + dppf<0x106>(d0[3]),
                         ((d1[0] + dppf<0x102>(d1[1])) + dppf<0x104>(d1[2])) + dppf<0x106>(d1[3])), 0xFFFFFFFEu);
          asm volatile("" ::: "memory");
          if (!__any(m == 0xFFFFFFFFu) || cx.dead) break;
          if ((spins & 63u) == 63u) {
            if (spins > 20000u) __hip_atomic_store(cx.abortw, 1u, RLX_AGENT);
            if (__builtin_amdgcn_readfirstlane(__hip_atomic_load(cx.abortw, RLX_AGENT))) cx.dead = true;
          }
        }
        if ((lane & 6) == 0) st4(ov, rx, ooff, nbase);
        SB();
        if (w == 0) {
          const unsigned cpub = cy32();
          if (lane == 0) st4(min(ck.time_of(cpub), 0xFFFFFFFEu), rx, xmap(XT2 + cg * 16), nbase);
          ldsst<unsigned>(L0 + L2_MISC, cpub);
          RD(32 + cg, s, 0, g2 - d_rdy); RD(32 + cg, s, 1, cpub - g2);
        }
      }
      barx();
      tref = __builtin_amdgcn_readfirstlane(ldsld<unsigned>(L0 + L2_MISC));
    }
    if (w == 3) {
#pragma unroll
      for (int u = 0; u < 4; ++u) st16(ldsld<v4u>(L0 + L2_UM + u * 1024 + lane * 16), rum, cg * 4096 + u * 1024 + lane * 16, s * (80 * 4096));
      ufv = ld4(cx.rc, ufo, 0);
    }
  }
  if (w == 3) { VMWAIT(0); if (lane == 0) flag_set(cx, C_MF2 + cg * 4, NSEG); }
}

enum { R_K = 0, R_V = 1, R_Q = 2, R_F1 = 3 };
constexpr int RING = 40960, Q_PART = 3 * RING;
constexpr int K_LEAD = 6, U_LEAD = 6, V_LEAD = 9;

__device__ __forceinline__ void load_w192(const u16* W, int ldw, v4u (&wr)[20][3], unsigned L0, int w, int lane) {
  const unsigned trb = (8 * (lane >> 4) + ((lane & 15) >> 2)) * 384 + (3 * w) * 32 + (lane & 3) * 8;
  stage384((const char*)W, ldw * 2, 0, 20, L0, w, lane);
  stage384((const char*)W, ldw * 2, 160, 20, L0 + 61440, w, lane);
#pragma unroll
  for (int c = 0; c < 4; ++c) {
    if (c < 3) VMWAIT(15); else VMWAIT(0);
    barx();
#pragma unroll
    for (int ks = 0; ks < 5; ++ks)
#pragma unroll
      for (int t = 0; t < 3; ++t) { const unsigned ad = L0 + (c & 1) * 61440 + trb + ks * 32 * 384 + t * 32; wr[5 * c + ks][t] = cat(trrd(ad), trrd(ad + 4 * 384)); }
    barx();
    if (c < 2) stage384((const char*)W, ldw * 2, 160 * (c + 2), 20, L0 + (c & 1) * 61440, w, lane);
  }
}
__device__ __forceinline__ void ring_issue(const char* src, int s, unsigned L0, int w) {
  const char* p = src + (size_t)s * RING;
  const unsigned d = L0 + (s % 3) * RING + w * 10240;
  dma16x4(p, d); dma16x4(p + 4096, d + 4096); dma16x2(p + 8192, d + 8192);
}
__device__ __forceinline__ void stagger(int w) {
  for (int i = 0; i < w; ++i) asm volatile("s_nop 7\n\ts_nop 3");
}
template <int OFF> __device__ __forceinline__ void dma16o(const char* gsrc) {
  asm volatile("global_load_lds_dwordx4 %0, off offset:%1 sc1" :: "v"(gsrc), "i"(OFF) : "memory");
}
__device__ __forceinline__ void set_m0(unsigned v) { asm volatile("s_mov_b32 m0, %0\n\ts_nop 0" :: "s"(v) : "memory"); }
__device__ __forceinline__ void gemm192(const v4u (&wr)[20][3], unsigned rb, f32x4 (&acc)[2][3], bool fill, const char* fsrc, unsigned fdst) {
  const f32x4 Z4 = {0.f, 0.f, 0.f, 0.f};
#pragma unroll
  for (int mt = 0; mt < 2; ++mt)
#pragma unroll
    for (int t = 0; t < 3; ++t) acc[mt][t] = Z4;
  stagger(__builtin_amdgcn_readfirstlane(threadIdx.x >> 6));
  v4u hb[4];
  hb[0] = ldsld<v4u>(rb); hb[1] = ldsld<v4u>(rb + 20480); hb[2] = ldsld<v4u>(rb + 1024); hb[3] = ldsld<v4u>(rb + 20480 + 1024);
#pragma unroll
  for (int ks = 0; ks < 20; ++ks)
#pragma unroll
    for (int mt = 0; mt < 2; ++mt) {
      const v4u hv = hb[(2 * ks + mt) & 3];
#pragma unroll
      for (int t = 0; t < 3; ++t) acc[mt][t] = mf32(wr[ks][t], hv, acc[mt][t]);
      SB();
      if (ks + 2 < 20) hb[(2 * ks + mt) & 3] = ldsld<v4u>(rb + mt * 20480 + (ks + 2) * 1024);
      if (mt == 1 && (ks & 1) == 0 && fill) {
        if ((ks & 7) == 0) set_m0(fdst + (ks >> 3) * 4096);
        const char* fp = fsrc + (ks >> 3) * 4096;
        if ((ks & 7) == 0) dma16o<0>(fp); else if ((ks & 7) == 2) dma16o<1024>(fp); else if ((ks & 7) == 4) dma16o<2048>(fp); else dma16o<3072>(fp);
      }
      SB();
    }
}

constexpr int PU_LDS = 3 * RING, PU_MISC = PU_LDS + 32768;
__device__ __forceinline__ void pu_task(const Args& a, const int task, const unsigned L0, Ctx& cx) {
  const int tid = threadIdx.x, lane = tid & 63, w = __builtin_amdgcn_readfirstlane(tid >> 6);
  const rsrc_t rux = mkrs(a.ws + O_UX, (size_t)S * H * DM * 2), rum = mkrs(a.ws + O_UM, (size_t)NSEG * 80 * 4096), rx1 = mkrs(a.ws + O_X1, SZ_ACT);
  const rsrc_t rpx = mkrs(a.ws + O_PX, (size_t)NSEG * 65536), rpm = mkrs(a.ws + O_PM, (size_t)NSEG * 16384);
  const float* mod = (const float*)(a.ws + O_MOD);
  const unsigned P0 = L0 + PU_LDS;
  const f32x4 Z4 = {0.f, 0.f, 0.f, 0.f};
  const int s = task / 10, part = task % 10;
  const unsigned lo = (((lane & 15) >> 3) + part * 8) , li = (lane & 1) * 1024 + (4 * ((lane & 7) >> 1) + (lane >> 4)) * 16;
  const unsigned uxo = lo * 16384 + li + w * 4096, umo = lo * 4096 + li + (w >> 1) * 2048 + (w & 1) * 512;
  const unsigned usb = s * (80 * 16384);
  const int col = part * 64 + w * 16 + 4 * (lane >> 4);
  const f32x4 g = *(const f32x4*)(mod + 2 * DM + col);
  const v2u xr0 = *(const v2u*)(a.x + (size_t)(s * 32 + (lane & 15)) * DM + col), xr1 = *(const v2u*)(a.x + (size_t)(s * 32 + 16 + (lane & 15)) * DM + col);
  v4u u[2][2][4];
#define PU_LDU(buf, k2)                                                                                                     \
  _Pragma("unroll") for (int k = 0; k < 2; ++k) _Pragma("unroll") for (int nt = 0; nt < 4; ++nt)                            \
    u[buf][k][nt] = (k2) < 4 ? ld16(rux, uxo + nt * 32768 + (((k2) * 2 + k) >> 2) * 2048 + (((k2) * 2 + k) & 3) * 256, usb) \
                             : ld16(rum, umo + nt * 8192 + k * 256, s * (80 * 4096));
  v4u p[10][2];
#define PU_LDP(k0, k1)                                                                                                      \
  _Pragma("unroll") for (int k = k0; k < k1; ++k) _Pragma("unroll") for (int mt = 0; mt < 2; ++mt)                          \
    p[k][mt] = k < 8 ? ld16(rpx, (mt * 32 + 8 * w + k) * 1024 + lane * 16, s * 65536) : ld16(rpm, (mt * 8 + 2 * w + k - 8) * 1024 + lane * 16, s * 16384);
  PU_LDP(0, 4)
  flags_wait(cx, lane < 8 ? (C_UF + (8 * w + lane) * 4) * 4 : (C_MF2 + (part * 8 + (lane & 7)) * 4) * 4, s + 1);
  PU_LDU(0, 0) PU_LDU(1, 1) PU_LDP(4, 10)
#undef PU_LDP
  SB();
  f32x4 d[2][4];
#pragma unroll
  for (int mt = 0; mt < 2; ++mt)
#pragma unroll
    for (int nt = 0; nt < 4; ++nt) d[mt][nt] = Z4;
#define PU_MM(buf, k2)                                                                                                      \
  _Pragma("unroll") for (int k = 0; k < 2; ++k) _Pragma("unroll") for (int nt = 0; nt < 4; ++nt) {                          \
    d[0][nt] = mf32(u[buf][k][nt], p[(k2) * 2 + k][0], d[0][nt]); d[1][nt] = mf32(u[buf][k][nt], p[(k2) * 2 + k][1], d[1][nt]); } SB();
  PU_MM(0, 0) PU_LDU(0, 2) SB();
  PU_MM(1, 1) PU_LDU(1, 3) SB();
  PU_MM(0, 2) PU_LDU(0, 4) SB();
  PU_MM(1, 3)
  PU_MM(0, 4)
#undef PU_LDU
#undef PU_MM
#pragma unroll
  for (int mt = 0; mt < 2; ++mt)
#pragma unroll
    for (int nt = 0; nt < 4; ++nt) ldsst<f32x4>(P0 + ((w * 4 + nt) * 2 + mt) * 1024 + lane * 16, d[mt][nt]);
  barx();
#pragma unroll
  for (int mt = 0; mt < 2; ++mt) {
    const unsigned pa = P0 + (w * 2 + mt) * 1024 + lane * 16;
    const f32x4 t = (ldsld<f32x4>(pa) + ldsld<f32x4>(pa + 8192)) + (ldsld<f32x4>(pa + 16384) + ldsld<f32x4>(pa + 24576));
    const v2u xr = mt ? xr1 : xr0;
    st8((v2u){cvtpk(blo(xr.x) + rbf(g[0] * rbf(t[0])), bhi(xr.x) + rbf(g[1] * rbf(t[1]))),
              cvtpk(blo(xr.y) + rbf(g[2] * rbf(t[2])), bhi(xr.y) + rbf(g[3] * rbf(t[3])))}, rx1, ((s * 32 + mt * 16 + (lane & 15)) * DM + col) * 2, 0);
  }
  VMWAIT(0); barx();
  if (tid == 0) ldsst<unsigned>(L0 + PU_MISC, __hip_atomic_fetch_add((gu32*)(a.ws + O_CTL) + C_XC + s * 32, 1u, RLX_AGENT));
  barx();
  if (__builtin_amdgcn_readfirstlane(ldsld<unsigned>(L0 + PU_MISC)) == 9u) {
    ln_segment<true>(a.ws + O_X1 + (size_t)s * 32 * DM * 2, a.ws + O_MODP + 2 * DM * 2, mkrs(a.ws + O_H2X, SZ_ACT), s * RING, w, lane);
    VMWAIT(0); barx();
    if (tid == 0) flag_set(cx, C_HG2 + s * 4, 1u);
  }
}
__device__ __forceinline__ int pu_claim(const Args& a, const unsigned L0, Ctx& cx, int h, int limit, int& seg, int& prog) {
  const int tid = threadIdx.x, lane = tid & 63;
  if (tid < 64) {
    const int sg = min(seg + (lane >> 1), NSEG - 1);
    const unsigned v = ld4(cx.rc, lane < 8 ? (C_PT + sg * 4 + (lane & 1)) * 4 : (C_MF1 + h * 32) * 4, 0);
    const unsigned pr = __builtin_amdgcn_readlane(v, 8) >> 2;
    const unsigned open = (unsigned)__ballot(lane < 8 && !(lane & 1) && v < 10u && seg + (lane >> 1) < NSEG);
    const unsigned rdy = (unsigned)__ballot(lane < 8 && (lane & 1) && v != 0u) >> 1;
    const int k = open ? __builtin_ctz(open) : 8;
    int nseg = seg + (k >> 1), t = nseg >= NSEG ? -2 : (open ? -1 : -3);
    if (open && ((rdy >> k) & 1u) && (int)pr <= limit && lane == 0) {
      const unsigned part = __hip_atomic_fetch_add((gu32*)(a.ws + O_CTL) + C_PT + nseg * 4, 1u, RLX_AGENT);
      if (part < 10u) t = nseg * 10 + (int)part; else t = -3;
    }
    if (lane == 0) ldsst<v4u>(L0 + PU_MISC + 32, (v4u){(unsigned)t, pr, (unsigned)nseg, 0u});
  }
  barx();
  const v4u r = ldsld<v4u>(L0 + PU_MISC + 32);
  prog = __builtin_amdgcn_readfirstlane(r.y);
  seg = __builtin_amdgcn_readfirstlane(r.z);
  const int t = (int)__builtin_amdgcn_readfirstlane(r.x);
  barx();
  return t;
}
__device__ __forceinline__ void gap_work(const Args& a, const unsigned L0, Ctx& cx, int h, int s, int lead, int& seg) {
#pragma unroll 1
  for (int it = 0; it < 60000; ++it) {
    int prog;
    const int t = pu_claim(a, L0, cx, h, s - lead, seg, prog);
    if (t >= 0) { pu_task(a, t, L0, cx); continue; }
    if (t == -3) continue;
    if (prog + lead > s || (t == -2 && s >= NSEG)) break;
    __builtin_amdgcn_s_sleep(10);
    if ((it & 255) == 255 && __builtin_amdgcn_readfirstlane(__hip_atomic_load(cx.abortw, RLX_AGENT))) break;
  }
}
__device__ __forceinline__ bool is_ahead(unsigned mnow, int s, int lead, const unsigned L0) {
  if (threadIdx.x == 0) ldsst<unsigned>(L0 + PU_MISC + 16, mnow);
  barx();
  return (int)(__builtin_amdgcn_readfirstlane(ldsld<unsigned>(L0 + PU_MISC + 16)) >> 2) + lead <= s;
}

template <int ROLE>
__device__ __forceinline__ void stream_kv(const Args& a, const int u, const unsigned L0, Ctx& cx) {
  const int tid = threadIdx.x, lane = tid & 63, w = __builtin_amdgcn_readfirstlane(tid >> 6);
  const int LEAD = ROLE == R_K ? a.lk : a.lv;
  v4u wr[20][3];
  load_w192((ROLE == R_K ? a.Wk : a.Wv) + u * DH, HD, wr, L0, w, lane);
  const char* src = a.ws + O_HX + w * 10240 + lane * 16;
  const rsrc_t rdst = mkrs(a.ws + (ROLE == R_K ? O_KX : O_VX), (size_t)S * HD * 2);
  const unsigned fword = (ROLE == R_K ? C_KF : C_VF) + u * 32;
  const char* mfp = a.ws + O_CTL + (C_MF1 + u * 32) * 4;
  STAMP(cx.slot, 1);
  int known = 0;
  seg_wait<C_HF1>(cx, 0, known, lane); ring_issue(src, 0, L0, w);
  seg_wait<C_HF1>(cx, 1, known, lane); ring_issue(src, 1, L0, w);
  bool drained = true;
  unsigned mprev = 0;
#pragma unroll 1
  for (int s = 0; s < NSEG; ++s) {
    if (s == 0) VMWAIT(10);
    else if (drained || s == NSEG - 1) VMWAIT(0);
    else if (ROLE == R_K) VMWAIT(13);
    else VMWAIT(14);
    PIN(mprev);
    if (tid == 0) ldsst<unsigned>(L0 + PU_MISC + 16 + (s & 1) * 4, mprev);
    barx();
    if (!drained && tid == 0) flag_set(cx, fword, s == NSEG - 1 ? s : s - 1);
    const bool ahead = (int)(__builtin_amdgcn_readfirstlane(ldsld<unsigned>(L0 + PU_MISC + 16 + (s & 1) * 4)) >> 2) + LEAD <= s;
    if (s + 2 < NSEG) seg_wait<C_HF1>(cx, s + 2, known, lane);
    mprev = ld4_async(mfp);
    f32x4 acc[2][3];
    gemm192(wr, L0 + (s % 3) * RING + lane * 16, acc, s + 2 < NSEG, src + (size_t)(s + 2) * RING, L0 + ((s + 2) % 3) * RING + w * 10240);
    const unsigned sb = s * (H * 12288);
    if (ROLE == R_K) {
      const unsigned o = (u * 4 + w) * 3072 + lane * 16;
      st16(cat(pack4(acc[0][0]), pack4(acc[0][1])), rdst, o, sb);
      st16(cat(pack4(acc[1][0]), pack4(acc[1][1])), rdst, o + 1024, sb);
      st16(cat(pack4(acc[0][2]), pack4(acc[1][2])), rdst, o + 2048, sb);
    } else {
      const unsigned o = u * 12288 + lane * 16;
#pragma unroll
      for (int mt = 0; mt < 2; ++mt) {
        st16(cat(pack4(acc[mt][0]), pack4(acc[mt][1])), rdst, o + mt * 6144 + w * 1024, sb);
        st8(pack4(acc[mt][2]), rdst, o + mt * 6144 + (4 + (w >> 1)) * 1024 + (w & 1) * 8, sb);
      }
    }
    drained = s < 2 || s == NSEG - 1 || ahead;
    if (drained) {
      VMWAIT(0); barx();
      if (tid == 0) flag_set(cx, fword, s + 1);
    }
    STAMP(cx.slot, 8 + s);
    if (ahead && s < NSEG - 1) { PIN(mprev); gap_work(a, L0, cx, u, s, LEAD, cx.seg); mprev = 0xFFFFu; }
  }
}

__device__ __forceinline__ void stream_f1(const Args& a, const int u, const unsigned L0, Ctx& cx) {
  const int tid = threadIdx.x, lane = tid & 63, w = __builtin_amdgcn_readfirstlane(tid >> 6);
  v4u wr[20][3];
  load_w192(a.w1 + u * DH, FF, wr, L0, w, lane);
  const char* src = a.ws + O_H2X + w * 10240 + lane * 16;
  const char* hfp = a.ws + O_CTL + (C_HG2 + lane * 4) * 4;
  const rsrc_t rdst = mkrs(a.ws + O_FX, (size_t)S * FF * 2);
  float bias[3][4];
#pragma unroll
  for (int t = 0; t < 3; ++t)
#pragma unroll
    for (int r = 0; r < 4; ++r) bias[t][r] = bf2f(a.b1[u * DH + 16 * (3 * w + t) + 4 * (lane >> 4) + r]);
  STAMP(cx.slot, 1);
  int issued = 0, known = 0;
  bool owe = false;
#pragma unroll 1
  for (int s = 0; s < NSEG; ++s) {
    if (issued == s) { seg_wait<C_HG2>(cx, s, known, lane); ring_issue(src, s, L0, w); issued = s + 1; VMWAIT(0); }
    barx();
    const bool fill = issued < known && issued <= s + 2;
    unsigned fv = ld4_async(hfp);
    f32x4 acc[2][3];
    gemm192(wr, L0 + (s % 3) * RING + lane * 16, acc, fill, src + (size_t)issued * RING, L0 + (issued % 3) * RING + w * 10240);
    if (fill) ++issued;
#pragma unroll
    for (int mt = 0; mt < 2; ++mt)
#pragma unroll
      for (int t = 0; t < 3; ++t)
#pragma unroll
        for (int r = 0; r < 4; r += 2) {
          const unsigned p = cvtpk(acc[mt][t][r], acc[mt][t][r + 1]);
          const unsigned q = cvtpk(blo(p) + bias[t][r], bhi(p) + bias[t][r + 1]);
          const float v0 = __builtin_amdgcn_fmed3f(blo(q), 0.f, 3.4e38f), v1 = __builtin_amdgcn_fmed3f(bhi(q), 0.f, 3.4e38f);
          acc[mt][t][r] = v0 * v0; acc[mt][t][r + 1] = v1 * v1;
        }
    VMWAIT(0);
    PIN(fv);
    if (owe && lane == 0) __hip_atomic_fetch_add((gu32*)(a.ws + O_CTL) + C_FF + u * 4, 1u, RLX_AGENT);
    { const unsigned long long miss = __ballot(fv == 0u); known = miss ? __builtin_ctzll(miss) : 64; }
    if (issued == s + 1 && issued < known) { ring_issue(src, issued, L0, w); ++issued; }
    const unsigned o = u * 12288 + lane * 16, sb = s * (8 * 12288);
#pragma unroll
    for (int mt = 0; mt < 2; ++mt) {
      st16(cat(pack4(acc[mt][0]), pack4(acc[mt][1])), rdst, o + mt * 6144 + w * 1024, sb);
      st8(pack4(acc[mt][2]), rdst, o + mt * 6144 + (4 + (w >> 1)) * 1024 + (w & 1) * 8, sb);
    }
    owe = fill && issued > s + 1;
    if (!owe) {
      VMWAIT(0);
      if (lane == 0) __hip_atomic_fetch_add((gu32*)(a.ws + O_CTL) + C_FF + u * 4, 1u, RLX_AGENT);
    }
    STAMP(cx.slot, 8 + s);
  }
}

__device__ __forceinline__ unsigned q_arrive_async(const Args& a, int s) {
  unsigned v = 1u;
  asm volatile("global_atomic_add %0, %1, %0, off sc0" : "+v"(v) : "v"(a.ws + O_CTL + (C_XC + s * 32 + 16) * 4) : "memory");
  return v;
}
__device__ __forceinline__ void stream_q(const Args& a, const int u, const unsigned L0, Ctx& cx) {
  const int tid = threadIdx.x, lane = tid & 63, w = __builtin_amdgcn_readfirstlane(tid >> 6);
  v4u wr[20][3];
  load_w192(a.Wq + u * DH, HD, wr, L0, w, lane);
  const char* src = a.ws + O_HX + w * 10240 + lane * 16;
  const rsrc_t rpx = mkrs(a.ws + O_PX, (size_t)NSEG * 65536), rpm = mkrs(a.ws + O_PM, (size_t)NSEG * 16384);
  const rsrc_t rk = mkrs(a.ws + O_KX, (size_t)S * HD * 2), rkm = mkrs(a.ws + O_KM, (size_t)NSEG * H * 6144);
  const unsigned kfo = (C_KF + u * 32) * 4, mfo = (C_MF1 + u * 32) * 4, ko = (u * 4 + w) * 3072 + lane * 16, mo = (u * 4 + w) * 1536;
  const unsigned fo = (lane & 1) ? kfo : mfo;
  const int smt = w >> 1, g = lane & 7, c16 = 8 * (w & 1) + (lane >> 3), last = 16 * smt + c16 - 4 * g;
  const unsigned pbuf = L0 + Q_PART;
  const unsigned rxa = pbuf + (smt * 3 + (g >> 2)) * 1024 + ((g & 3) * 16 + c16) * 16;
  const unsigned rma = pbuf + (smt * 3 + 2) * 1024 + ((2 + (g >> 2)) * 16 + c16) * 16 + (g & 3) * 4;
  const unsigned sxo = (smt * 32 + u) * 1024 + ((g & 3) * 16 + c16) * 16 + (g >> 2) * 8;
  const unsigned smo = (smt * 8 + (u >> 2)) * 1024 + ((u & 3) * 16 + c16) * 16 + g * 2;
  const f32x4 Z4 = {0.f, 0.f, 0.f, 0.f};
  STAMP(cx.slot, 1);
  int known = 0;
  seg_wait<C_HF1>(cx, 2, known, lane);
  ring_issue(src, 0, L0, w); ring_issue(src, 1, L0, w);
  v4u q01[2]; v2u q2[2];
  unsigned kready = 0, mready = 0, fv = 0, tk = 0;
  int owe = -1, pend = -1;
#pragma unroll 1
  for (int s = -1; s < NSEG; ++s) {
    v4u k0, k1, k2, m01; v2u m2;
    const bool early = s >= 0 && mready >= 4u * (s + 1);
    if (s >= 0) {
      if (kready < (unsigned)s + 1u) flags_wait(cx, kfo, s + 1);
      k0 = ld16(rk, ko, s * (H * 12288)); k1 = ld16(rk, ko + 1024, s * (H * 12288)); k2 = ld16(rk, ko + 2048, s * (H * 12288));
      if (early) { m01 = ld16(rkm, mo + lane * 16, s * (H * 6144)); m2 = ld8(rkm, mo + 1024 + lane * 8, s * (H * 6144)); }
    }
    v4u n01[2]; v2u n2[2];
    if (s + 1 < NSEG) {
      if (s < 0) VMWAIT(10);
      barx();
      if (s + 3 < NSEG) seg_wait<C_HF1>(cx, s + 3, known, lane);
      f32x4 acc[2][3];
      gemm192(wr, L0 + ((s + 1) % 3) * RING + lane * 16, acc, s + 3 < NSEG, src + (size_t)(s + 3) * RING, L0 + ((s + 3) % 3) * RING + w * 10240);
      fv = ld4_async(a.ws + O_CTL + fo);
#pragma unroll
      for (int mt = 0; mt < 2; ++mt) { n01[mt] = cat(pack4(acc[mt][0]), pack4(acc[mt][1])); n2[mt] = pack4(acc[mt][2]); }
    }
    VMWAIT(0);
    PIN(fv);
    mready = __builtin_amdgcn_readlane(fv, 0); kready = __builtin_amdgcn_readlane(fv, 1);
    PIN(tk);
    if (pend >= 0 && tid == 0 && tk == 31u) flag_set(cx, C_PT + pend * 4 + 1, 1u);
    pend = -1;
    if (s >= 0) {
      if (tid == 0) ldsst<unsigned>(pbuf + 24576, mready);
      if (!early) {
        if (mready < 4u * (s + 1)) flags_wait(cx, mfo, 4 * (s + 1));
        m01 = ld16(rkm, mo + lane * 16, s * (H * 6144)); m2 = ld8(rkm, mo + 1024 + lane * 8, s * (H * 6144));
      }
      const v4u qa0 = {q2[0].x, q2[0].y, 0u, 0u}, qa1 = {q2[1].x, q2[1].y, 0u, 0u}, qb1 = {0u, 0u, q2[1].x, q2[1].y}, mz = {m2.x, m2.y, 0u, 0u};
      f32x4 t0 = mf32(k0, q01[0], Z4), t1 = mf32(k0, q01[1], Z4), t2 = mf32(k1, q01[1], Z4), t3 = mf32(m01, q01[0], Z4), t4 = mf32(m01, q01[1], Z4);
      t0 = mf32(k2, qa0, t0); t1 = mf32(k2, qa1, t1); t2 = mf32(k2, qb1, t2); t3 = mf32(mz, qa0, t3); t4 = mf32(mz, qa1, t4);
      const unsigned pa = pbuf + w * 6144 + lane * 16;
      ldsst<f32x4>(pa, t0); ldsst<f32x4>(pa + 3072, t1); ldsst<f32x4>(pa + 4096, t2); ldsst<f32x4>(pa + 2048, t3); ldsst<f32x4>(pa + 5120, t4);
      barx();
      if (owe >= 0) { if (tid == 0) tk = q_arrive_async(a, owe); pend = owe; owe = -1; }
      const bool behind = __builtin_amdgcn_readfirstlane(ldsld<unsigned>(pbuf + 24576)) >= 4u * (s + 2) && s + 2 < NSEG;
      const f32x4 xs = (ldsld<f32x4>(rxa) + ldsld<f32x4>(rxa + 6144)) + (ldsld<f32x4>(rxa + 12288) + ldsld<f32x4>(rxa + 18432));
      const float ms = (ldsld<float>(rma) + ldsld<float>(rma + 6144)) + (ldsld<float>(rma + 12288) + ldsld<float>(rma + 18432));
      const unsigned a0 = cvtpk(xs[0], xs[1]), a1 = cvtpk(xs[2], xs[3]), a2 = cvtpk(ms, ms);
      const unsigned b0 = cvtpk(blo(a0) * SCALE, bhi(a0) * SCALE), b1 = cvtpk(blo(a1) * SCALE, bhi(a1) * SCALE), b2 = cvtpk(blo(a2) * SCALE, 0.f);
      const float NEG = -3.0e38f;
      const float v0 = last >= 0 ? blo(b0) : NEG, v1 = last >= 1 ? bhi(b0) : NEG, v2 = last >= 2 ? blo(b1) : NEG, v3 = last >= 3 ? bhi(b1) : NEG, v4 = blo(b2);
      float mx = __builtin_amdgcn_fmed3f(__builtin_amdgcn_fmed3f(v0, v1, 3.4e38f), __builtin_amdgcn_fmed3f(v2, v3, 3.4e38f), 3.4e38f);
      mx = __builtin_amdgcn_fmed3f(mx, v4, 3.4e38f);
      mx = __builtin_amdgcn_fmed3f(mx, dppf<0xB1>(mx), 3.4e38f);
      mx = __builtin_amdgcn_fmed3f(mx, dppf<0x4E>(mx), 3.4e38f);
      mx = __builtin_amdgcn_fmed3f(mx, dppf<0x141>(mx), 3.4e38f);
      const float e0 = __builtin_amdgcn_exp2f((v0 - mx) * LOG2E), e1 = __builtin_amdgcn_exp2f((v1 - mx) * LOG2E), e2 = __builtin_amdgcn_exp2f((v2 - mx) * LOG2E),
                  e3 = __builtin_amdgcn_exp2f((v3 - mx) * LOG2E), e4 = __builtin_amdgcn_exp2f((v4 - mx) * LOG2E);
      float sum = ((e0 + e1) + (e2 + e3)) + e4;
      sum += dppf<0xB1>(sum); sum += dppf<0x4E>(sum); sum += dppf<0x141>(sum);
      const float inv = __builtin_amdgcn_rcpf(sum);
      st8((v2u){cvtpk(e0 * inv, e1 * inv), cvtpk(e2 * inv, e3 * inv)}, rpx, sxo, s * 65536);
      const float pm = e4 * inv, po = dppf<0xB1>(pm);
      if (!(g & 1)) st4(cvtpk(pm, po), rpm, smo, s * 16384);
      if (behind) owe = s;
      else {
        VMWAIT(0);
        PIN(tk);
        barx();
        if (tid == 0) {
          if (pend >= 0 && tk == 31u) flag_set(cx, C_PT + pend * 4 + 1, 1u);
          if (__hip_atomic_fetch_add((gu32*)(a.ws + O_CTL) + C_XC + s * 32 + 16, 1u, RLX_AGENT) == 31u) flag_set(cx, C_PT + s * 4 + 1, 1u);
        }
        pend = -1;
      }
      STAMP(cx.slot, 8 + s);
    }
#pragma unroll
    for (int mt = 0; mt < 2; ++mt) { q01[mt] = n01[mt]; q2[mt] = n2[mt]; }
  }
}

__device__ __forceinline__ void unit_u(const Args& a, const int h, const unsigned L0, Ctx& cx) {
  const int tid = threadIdx.x, lane = tid & 63, w = __builtin_amdgcn_readfirstlane(tid >> 6);
  v2u wh[12][10];
  {
    const char* W = (const char*)(a.Wo + (size_t)h * DH * DM);
    const unsigned trb = (4 * (lane >> 4) + ((lane & 15) >> 2)) * 1280 + w * 320 + (lane & 3) * 8;
    auto stage = [&](int c) {
#pragma unroll 1
      for (int i = w; i < 60; i += 4) dma16p(W + c * 61440 + i * 1024 + lane * 16, L0 + (c & 1) * 61440 + i * 1024);
    };
    stage(0); stage(1);
#pragma unroll
    for (int c = 0; c < 4; ++c) {
      if (c < 3) VMWAIT(15); else VMWAIT(0);
      barx();
#pragma unroll
      for (int t = 0; t < 3; ++t)
#pragma unroll
        for (int nt = 0; nt < 10; ++nt) wh[3 * c + t][nt] = trrd(L0 + (c & 1) * 61440 + trb + t * 16 * 1280 + nt * 32);
      barx();
      if (c < 2) stage(c + 2);
    }
  }
  const char* src = a.ws + O_VX + (size_t)h * 12288 + w * 3072 + lane * 16;
  const rsrc_t rdst = mkrs(a.ws + O_UX, (size_t)S * H * DM * 2);
  const unsigned vfo = (C_VF + h * 32) * 4;
  const unsigned so = ((lane & 15) >> 3) * 16384 + (h >> 2) * 2048 + (lane & 1) * 1024 + ((h & 3) * 16 + 4 * ((lane & 7) >> 1) + (lane >> 4)) * 16 + w * (20 * 16384);
  auto issue = [&](int s) {
    dma16x2(src + (size_t)s * (H * 12288), L0 + (s & 3) * 12288 + w * 3072);
    dma16(src + (size_t)s * (H * 12288) + 2048, L0 + (s & 3) * 12288 + w * 3072 + 2048);
  };
  STAMP(cx.slot, 1);
  int issued = 0;
  bool drained = true;
  unsigned mprev = 0;
  const f32x4 Z4 = {0.f, 0.f, 0.f, 0.f};
#pragma unroll 1
  for (int s = 0; s < NSEG; ++s) {
    if (issued == s) { flags_wait(cx, vfo, s + 1); issue(s); issued = s + 1; VMWAIT(0); }
    else if (drained) VMWAIT(0);
    else VMWAIT(5);
    PIN(mprev);
    if (tid == 0) ldsst<unsigned>(L0 + PU_MISC + 16 + (s & 1) * 4, mprev);
    barx();
    if (!drained && tid == 0) flag_set(cx, C_UF + h * 4, s - 1);
    const bool ahead = (int)(__builtin_amdgcn_readfirstlane(ldsld<unsigned>(L0 + PU_MISC + 16 + (s & 1) * 4)) >> 2) + a.lu <= s;
    mprev = ld4_async(a.ws + O_CTL + (C_MF1 + h * 32) * 4);
    const unsigned rb = L0 + (s & 3) * 12288 + lane * 16;
    v4u vf[2][6];
#pragma unroll
    for (int mt = 0; mt < 2; ++mt)
#pragma unroll
      for (int ks = 0; ks < 6; ++ks) vf[mt][ks] = ldsld<v4u>(rb + (mt * 6 + ks) * 1024);
    const unsigned vnow = ld4(cx.rc, vfo, 0);
#pragma unroll
    for (int nt = 0; nt < 10; ++nt) {
      f32x4 d0 = Z4, d1 = Z4;
#pragma unroll
      for (int ks = 0; ks < 6; ++ks) {
        const v4u b = cat(wh[ks < 4 ? 3 * ks : (ks == 4 ? 2 : 8)][nt], wh[ks < 4 ? 3 * ks + 1 : (ks == 4 ? 5 : 11)][nt]);
        d0 = mf32(vf[0][ks], b, d0);
        d1 = mf32(vf[1][ks], b, d1);
      }
      st16(cat(pack4(d0), pack4(d1)), rdst, so + nt * 32768, s * (80 * 16384));
      if (nt == 4 && s + 1 < NSEG && (unsigned)__builtin_amdgcn_readfirstlane(vnow) >= (unsigned)s + 2u) { issue(s + 1); issued = s + 2; }
    }
    drained = s < 2 || s == NSEG - 1 || ahead;
    if (drained) {
      VMWAIT(0); barx();
      if (tid == 0) flag_set(cx, C_UF + h * 4, s + 1);
    }
    STAMP(cx.slot, 8 + s);
    if (ahead && s < NSEG - 1) { PIN(mprev); gap_work(a, L0, cx, h, s, a.lu, cx.seg); mprev = 0xFFFFu; }
  }
}

__device__ __forceinline__ void unit_f2(const Args& a, const int jj, const unsigned L0, Ctx& cx) {
  const int tid = threadIdx.x, lane = tid & 63, w = __builtin_amdgcn_readfirstlane(tid >> 6);
  v2u wh[2][12][5];
  {
    const unsigned trb = w * 30720 + (4 * (lane >> 4) + ((lane & 15) >> 2)) * 160 + (lane & 3) * 8;
#pragma unroll
    for (int c = 0; c < 2; ++c) {
      for (int i = 0; i < 30; ++i) {
        const unsigned idx = i * 64 + lane, row = idx / 10, ch = idx % 10;
        dma16p(a.w2 + (size_t)(384 * w + 192 * c + row) * DM + jj * 80 + ch * 8, L0 + w * 30720 + i * 1024);
      }
      VMWAIT(0); barx();
#pragma unroll
      for (int t = 0; t < 12; ++t)
#pragma unroll
        for (int nt = 0; nt < 5; ++nt) wh[c][t][nt] = trrd(L0 + trb + t * 16 * 160 + nt * 32);
      barx();
    }
  }
  const rsrc_t rf = mkrs(a.ws + O_FX, (size_t)S * FF * 2), rx1 = mkrs(a.ws + O_X1, SZ_ACT);
  const unsigned ffo = (C_FF + (2 * w + (lane & 1)) * 4) * 4, fo = 2 * w * 12288 + lane * 16;
  v2u bb[3]; f32x4 gg[3]; unsigned oo[3];
#pragma unroll
  for (int k = 0; k < 3; ++k) {
    const int i = w + 4 * k < 10 ? w + 4 * k : w, col = jj * 80 + (i % 5) * 16 + 4 * (lane >> 4);
    bb[k] = *(const v2u*)(a.b2 + col);
    gg[k] = *(const f32x4*)((const float*)(a.ws + O_MOD) + 5 * DM + col);
    oo[k] = (((i / 5) * 16 + (lane & 15)) * DM + col) * 2;
  }
  STAMP(cx.slot, 1);
  const f32x4 Z4 = {0.f, 0.f, 0.f, 0.f};
  v4u fa[12], fb[12];
  bool have = false;
#pragma unroll 1
  for (int s = 0; s < NSEG; ++s) {
    const unsigned sb = s * (8 * 12288);
    if (!have) {
      flags_wait(cx, ffo, 4 * (s + 1));
#pragma unroll
      for (int i = 0; i < 12; ++i) fa[i] = ld16(rf, fo + i * 1024, sb);
    }
    const unsigned nf = ld4(cx.rc, ffo, 0);
    v2u xr[3];
    f32x4 acc[2][5];
#pragma unroll
    for (int mt = 0; mt < 2; ++mt)
#pragma unroll
      for (int nt = 0; nt < 5; ++nt) acc[mt][nt] = Z4;
    SB();
#pragma unroll
    for (int ks = 0; ks < 6; ++ks)
#pragma unroll
      for (int nt = 0; nt < 5; ++nt) {
        const v4u wa = cat(wh[0][ks < 4 ? 3 * ks : (ks == 4 ? 2 : 8)][nt], wh[0][ks < 4 ? 3 * ks + 1 : (ks == 4 ? 5 : 11)][nt]);
        acc[0][nt] = mf32(wa, fa[ks], acc[0][nt]);
        acc[1][nt] = mf32(wa, fa[6 + ks], acc[1][nt]);
        SB();
        if (nt == 0) fb[ks] = ld16(rf, fo + 12288 + ks * 1024, sb);
        if (nt == 2) fb[6 + ks] = ld16(rf, fo + 12288 + (6 + ks) * 1024, sb);
        if (nt == 4 && ks < 3) xr[ks] = ld8(rx1, oo[ks], s * (32 * DM * 2));
        SB();
      }
    have = s + 1 < NSEG && !__any(nf < 4u * (s + 2));
    const unsigned nsb = have ? sb + 8 * 12288 : sb;
    SB();
#pragma unroll
    for (int ks = 0; ks < 6; ++ks)
#pragma unroll
      for (int nt = 0; nt < 5; ++nt) {
        const v4u wa = cat(wh[1][ks < 4 ? 3 * ks : (ks == 4 ? 2 : 8)][nt], wh[1][ks < 4 ? 3 * ks + 1 : (ks == 4 ? 5 : 11)][nt]);
        acc[0][nt] = mf32(wa, fb[ks], acc[0][nt]);
        acc[1][nt] = mf32(wa, fb[6 + ks], acc[1][nt]);
        SB();
        if (nt == 1) fa[ks] = ld16(rf, fo + ks * 1024, nsb);
        if (nt == 3) fa[6 + ks] = ld16(rf, fo + (6 + ks) * 1024, nsb);
        SB();
      }
    const unsigned pb = L0 + (s & 1) * 40960;
#pragma unroll
    for (int mt = 0; mt < 2; ++mt)
#pragma unroll
      for (int nt = 0; nt < 5; ++nt) ldsst<f32x4>(pb + (w * 10 + mt * 5 + nt) * 1024 + lane * 16, acc[mt][nt]);
    barx();
#pragma unroll
    for (int k = 0; k < 3; ++k) {
      if (w + 4 * k < 10) {
        const unsigned pa = pb + (w + 4 * k) * 1024 + lane * 16;
        const f32x4 t = (ldsld<f32x4>(pa) + ldsld<f32x4>(pa + 10240)) + (ldsld<f32x4>(pa + 20480) + ldsld<f32x4>(pa + 30720));
        const float o0 = blo(xr[k].x) + rbf(gg[k][0] * rbf(rbf(t[0]) + blo(bb[k].x))), o1 = bhi(xr[k].x) + rbf(gg[k][1] * rbf(rbf(t[1]) + bhi(bb[k].x)));
        const float o2 = blo(xr[k].y) + rbf(gg[k][2] * rbf(rbf(t[2]) + blo(bb[k].y))), o3 = bhi(xr[k].y) + rbf(gg[k][3] * rbf(rbf(t[3]) + bhi(bb[k].y)));
        *(v2u*)((char*)a.out + (size_t)s * (32 * DM * 2) + oo[k]) = (v2u){cvtpk(o0, o1), cvtpk(o2, o3)};
      }
    }
    STAMP(cx.slot, 8 + s);
  }
}

__global__ void __launch_bounds__(256, 1) k_main(Args a) {
  extern __shared__ __attribute__((aligned(16))) char lds_[];
  const unsigned L0 = (unsigned)(size_t)(LAS char*)lds_;
  const int tid = threadIdx.x, lane = tid & 63, w = __builtin_amdgcn_readfirstlane(tid >> 6);
  if (tid == 0) {
    unsigned x = xcc_id() & 7u, slot = 0;
    for (int tries = 0; tries < 8; ++tries) {
      const unsigned rk = __hip_atomic_fetch_add((gu32*)(a.ws + O_CTL) + C_TICK + x * 32, 1u, RLX_AGENT);
      if (rk < 32u) { slot = x * 32u + rk; break; }
      x = (x + 1u) & 7u;
    }
    ldsst<unsigned>(L0, slot);
  }
  barx();
  const unsigned slot = __builtin_amdgcn_readfirstlane(ldsld<unsigned>(L0));
  barx();
  Ctx cx{mkrs(a.ws + O_CTL, 65536), (gu32*)(a.ws + O_CTL) + C_ABORT, false, slot, 0};
  STAMP(slot, 0);
  const unsigned x = slot >> 5, rk = slot & 31u;
  if (x < 4 && rk < 8) { layer1(a, x * 8 + rk, L0, cx); STAMP(slot, 2); barx(); gap_work(a, L0, cx, 0, 1000, 0, cx.seg); STAMP(slot, 3); return; }
  if (x < 4 && rk < 28) { layer2(a, x * 20 + (rk - 8), L0, cx); STAMP(slot, 2); barx(); gap_work(a, L0, cx, 0, 1000, 0, cx.seg); STAMP(slot, 3); return; }
  auto ln1 = [&](int seg) {
    ln_segment<false>((const char*)(a.x + (size_t)seg * 32 * DM), a.ws + O_MODP, mkrs(a.ws + O_HX, SZ_ACT), seg * RING, w, lane);
    VMWAIT(0); barx();
    if (tid == 0) flag_set(cx, C_HF1 + seg * 4, 1u);
  };
  if (x < 4) {
    const int f = x * 4 + (rk - 28);
    ln1(32 + 2 * f); ln1(33 + 2 * f);
    flags_wait(cx, C_UF * 4, 1u);
    if (f < 8) stream_f1(a, f, L0, cx); else unit_f2(a, f - 8, L0, cx);
    STAMP(slot, 2); STAMP(slot, 3);
    return;
  }
  const int h = (x - 4) * 8 + (rk & 7), kind = rk >> 3;
  if (kind == 3) { ln1(h); flags_wait(cx, (C_VF + h * 32) * 4, 1u); }
  if (kind == 0) stream_kv<R_K>(a, h, L0, cx);
  else if (kind == 1) stream_kv<R_V>(a, h, L0, cx);
  else if (kind == 2) unit_u(a, h, L0, cx);
  else { stream_q(a, h, L0, cx); STAMP(slot, 2); STAMP(slot, 3); return; }
  STAMP(slot, 2);
  barx();
  gap_work(a, L0, cx, h, 1000, 0, cx.seg);
  STAMP(slot, 3);
}

extern "C" void kernel_launch(void* const* d_in, const int* in_sizes, int n_in, void* d_out, int out_size, void* d_ws, size_t ws_size, hipStream_t stream) {
  if (ws_size < WS_NEED) return;
  Args a;
  a.x = (const u16*)d_in[0]; a.mem0 = (const u16*)d_in[1]; a.Wq = (const u16*)d_in[2]; a.Wk = (const u16*)d_in[3]; a.Wv = (const u16*)d_in[4]; a.Wo = (const u16*)d_in[5];
  a.w1 = (const u16*)d_in[8]; a.b1 = (const u16*)d_in[9]; a.w2 = (const u16*)d_in[10]; a.b2 = (const u16*)d_in[11]; a.c = (const u16*)d_in[12]; a.wmod = (const u16*)d_in[13];
  a.ws = (char*)d_ws; a.out = (u16*)d_out;
  a.w1q = 70;
  a.w2q = 80;
  a.l2d = 300; a.lk = 8; a.lu = 8; a.lv = 13;
  (void)hipFuncSetAttribute((const void*)k_main, hipFuncAttributeMaxDynamicSharedMemorySize, MAIN_LDS);
  hipLaunchKernelGGL(k_pre, dim3(PRE_GRID), dim3(256), 0, stream, a);
  hipLaunchKernelGGL(k_main, dim3(256), dim3(256), MAIN_LDS, stream, a);
}
```

```cpp
#include <hip/hip_runtime.h>
#include <hip/hip_bf16.h>
#include <stdint.h>
#include <stdlib.h>

typedef unsigned short u16;
typedef __bf16 bf16x2 __attribute__((ext_vector_type(2)));
typedef __bf16 bf16x4 __attribute__((ext_vector_type(4)));
typedef __bf16 bf16x8 __attribute__((ext_vector_type(8)));
typedef __bf16 bf16x16 __attribute__((ext_vector_type(16)));
typedef float f32x2 __attribute__((ext_vector_type(2)));
typedef float f32x4 __attribute__((ext_vector_type(4)));
typedef unsigned v2u __attribute__((ext_vector_type(2)));
typedef unsigned v4u __attribute__((ext_vector_type(4)));
typedef unsigned v8u __attribute__((ext_vector_type(8)));
#define GAS __attribute__((address_space(1)))
#define LAS __attribute__((address_space(3)))
typedef GAS unsigned gu32;
#define RLX_AGENT __ATOMIC_RELAXED, __HIP_MEMORY_SCOPE_AGENT
typedef __amdgpu_buffer_rsrc_t rsrc_t;

constexpr int S = 2048, DM = 640, H = 32, DH = 192, HD = H * DH, NSEG = 64, FF = 1536;
constexpr float SCALE = 0.07216878364870322f, LOG2E = 1.4426950408889634f;

constexpr unsigned XSTEP = 131072;
constexpr unsigned XP = 10240, XT1 = 30720, XT2 = 31232;
__host__ __device__ constexpr unsigned xmap(unsigned o) { return ((o >> 13) << 14) + (o & 8191u); }
constexpr size_t O_XR = 0, O_CTL = O_XR + (size_t)NSEG * XSTEP, O_SEL = O_CTL + 65536, O_MOD = O_SEL + 4096, O_MODP = O_MOD + 16384, O_HX = O_MODP + 8192;
constexpr size_t SZ_ACT = (size_t)S * DM * 2;
constexpr size_t O_H2X = O_HX + SZ_ACT, O_X1 = O_H2X + SZ_ACT, O_KX = O_X1 + SZ_ACT, O_VX = O_KX + (size_t)S * HD * 2, O_UX = O_VX + (size_t)S * HD * 2;
constexpr size_t O_UM = O_UX + (size_t)S * H * DM * 2, O_KM = O_UM + (size_t)NSEG * 80 * 4096, O_PX = O_KM + (size_t)NSEG * H * 6144;
constexpr size_t O_PM = O_PX + (size_t)NSEG * 65536, O_FX = O_PM + (size_t)NSEG * 16384, O_BT = O_FX + (size_t)S * FF * 2, O_DBG = O_BT + (size_t)H * DM * DM * 2;
constexpr size_t WS_NEED = O_DBG + (4u << 20);
constexpr int C_TICK = 0, C_ABORT = 256, C_LNC = 288, C_KF = 512, C_VF = 1536, C_MF1 = 2560, C_UF = 3584, C_PF = 3712, C_MF2 = 3840, C_XC = 4224,
              C_HF2 = 6272, C_FF = 8320, C_PUT = 8416, C_HF1 = 8448, C_HG2 = 8704, C_PT = 8960, C_SCH = 9216, C_WORDS = 9248;

struct Args {
  const u16 *x, *mem0, *Wq, *Wk, *Wv, *Wo, *w1, *b1, *w2, *b2, *c, *wmod;
  char* ws; u16* out;
  int w1q, w2q, l2d, lk, lu, lv;
};

__device__ __forceinline__ float bf2f(u16 v) { return __uint_as_float(((unsigned)v) << 16); }
__device__ __forceinline__ float blo(unsigned v) { return __uint_as_float(v << 16); }
__device__ __forceinline__ float bhi(unsigned v) { return __uint_as_float(v & 0xFFFF0000u); }
__device__ __forceinline__ unsigned cvtpk(float a, float b) { return __builtin_bit_cast(unsigned, __builtin_convertvector((f32x2){a, b}, bf16x2)); }
__device__ __forceinline__ float rbf(float f) { return blo(cvtpk(f, f)); }
template <typename T> __device__ __forceinline__ T ldsld(unsigned a) { return *(LAS T*)(size_t)a; }
template <typename T> __device__ __forceinline__ void ldsst(unsigned a, T v) { *(LAS T*)(size_t)a = v; }
#define SB() __builtin_amdgcn_sched_barrier(0)
__device__ __forceinline__ void barx() { __builtin_amdgcn_s_waitcnt(0xC07F); __builtin_amdgcn_s_barrier(); SB(); }
#define VMWAIT(n) asm volatile("s_waitcnt vmcnt(" #n ")" ::: "memory")
__device__ __forceinline__ rsrc_t mkrs(const void* p, size_t bytes) { return __builtin_amdgcn_make_buffer_rsrc((void*)p, 0, (unsigned)bytes, 0x00020000); }
__device__ __forceinline__ v4u ld16(rsrc_t r, unsigned vo, unsigned so) { return __builtin_amdgcn_raw_buffer_load_b128(r, vo, so, 16); }
__device__ __forceinline__ v2u ld8(rsrc_t r, unsigned vo, unsigned so) { return __builtin_amdgcn_raw_buffer_load_b64(r, vo, so, 16); }
__device__ __forceinline__ unsigned ld4(rsrc_t r, unsigned vo, unsigned so) { return __builtin_amdgcn_raw_buffer_load_b32(r, vo, so, 16); }
__device__ __forceinline__ void st16(v4u v, rsrc_t r, unsigned vo, unsigned so) {
  __builtin_amdgcn_raw_buffer_store_b128(v, r, vo, so, 16);
  asm volatile("s_nop 1" :: "v"(v));
}
__device__ __forceinline__ void st8(v2u v, rsrc_t r, unsigned vo, unsigned so) { __builtin_amdgcn_raw_buffer_store_b64(v, r, vo, so, 16); }
__device__ __forceinline__ void st4(unsigned v, rsrc_t r, unsigned vo, unsigned so) { __builtin_amdgcn_raw_buffer_store_b32(v, r, vo, so, 16); }
__device__ __forceinline__ f32x4 smf(v4u a, v8u b, f32x4 c, int idx) {
  return __builtin_amdgcn_smfmac_f32_16x16x64_bf16(__builtin_bit_cast(bf16x8, a), __builtin_bit_cast(bf16x16, b), c, idx, 0, 0);
}
__device__ __forceinline__ f32x4 mf32(v4u a, v4u b, f32x4 c) {
  return __builtin_amdgcn_mfma_f32_16x16x32_bf16(__builtin_bit_cast(bf16x8, a), __builtin_bit_cast(bf16x8, b), c, 0, 0, 0);
}
__device__ __forceinline__ v2u trrd(unsigned a) { return __builtin_bit_cast(v2u, __builtin_amdgcn_ds_read_tr16_b64_v4bf16((LAS bf16x4*)(size_t)a)); }
__device__ __forceinline__ v2u pack4(f32x4 v) { return (v2u){cvtpk(v[0], v[1]), cvtpk(v[2], v[3])}; }
__device__ __forceinline__ v4u cat(v2u a, v2u b) { return (v4u){a.x, a.y, b.x, b.y}; }
__device__ __forceinline__ v8u cat8(v4u a, v4u b) { return __builtin_shufflevector(a, b, 0, 1, 2, 3, 4, 5, 6, 7); }
__device__ __forceinline__ void dma16(const void* gsrc, unsigned lds_dst) {
  unsigned keep;
  asm volatile("s_mov_b32 %0, m0\n\ts_mov_b32 m0, %2\n\ts_nop 0\n\tglobal_load_lds_dwordx4 %1, off sc1\n\ts_mov_b32 m0, %0"
               : "=&s"(keep) : "v"(gsrc), "s"(lds_dst) : "memory");
}
__device__ __forceinline__ void dma16x4(const void* gsrc, unsigned lds_dst) {
  unsigned keep;
  asm volatile("s_mov_b32 %0, m0\n\ts_mov_b32 m0, %2\n\ts_nop 0\n\tglobal_load_lds_dwordx4 %1, off sc1\n\tglobal_load_lds_dwordx4 %1, off offset:1024 sc1\n\t"
               "global_load_lds_dwordx4 %1, off offset:2048 sc1\n\tglobal_load_lds_dwordx4 %1, off offset:3072 sc1\n\ts_mov_b32 m0, %0"
               : "=&s"(keep) : "v"(gsrc), "s"(lds_dst) : "memory");
}
__device__ __forceinline__ void dma16x2(const void* gsrc, unsigned lds_dst) {
  unsigned keep;
  asm volatile("s_mov_b32 %0, m0\n\ts_mov_b32 m0, %2\n\ts_nop 0\n\tglobal_load_lds_dwordx4 %1, off sc1\n\tglobal_load_lds_dwordx4 %1, off offset:1024 sc1\n\t"
               "s_mov_b32 m0, %0" : "=&s"(keep) : "v"(gsrc), "s"(lds_dst) : "memory");
}
__device__ __forceinline__ void dma16p(const void* gsrc, unsigned lds_dst) {
  unsigned keep;
  asm volatile("s_mov_b32 %0, m0\n\ts_mov_b32 m0, %2\n\ts_nop 0\n\tglobal_load_lds_dwordx4 %1, off\n\ts_mov_b32 m0, %0"
               : "=&s"(keep) : "v"(gsrc), "s"(lds_dst) : "memory");
}
__device__ __forceinline__ unsigned xcc_id() { return (unsigned)__builtin_amdgcn_s_getreg(20 | (3 << 11)) & 0xfu; }
__device__ __forceinline__ unsigned umax4(v4u v) { return max(max(v.x, v.y), max(v.z, v.w)); }
__device__ __forceinline__ unsigned rt32() { return (unsigned)__builtin_amdgcn_s_memrealtime(); }
__device__ __forceinline__ unsigned cy32() { return (unsigned)__builtin_amdgcn_s_memtime(); }
__device__ __forceinline__ void wait_cy(unsigned c) { while ((int)(cy32() - c) < 0) {} }
template <int CTRL> __device__ __forceinline__ float dppf(float v) {
  return __builtin_bit_cast(float, __builtin_amdgcn_update_dpp(0, __builtin_bit_cast(int, v), CTRL, 0xf, 0xf, true));
}
__device__ __forceinline__ float row16_sum(float x) { x += dppf<0xB1>(x); x += dppf<0x4E>(x); x += dppf<0x141>(x); x += dppf<0x140>(x); return x; }
__device__ __forceinline__ float xq_max(float x) {
  auto s = __builtin_amdgcn_permlane16_swap(__float_as_uint(x), __float_as_uint(x), false, false);
  x = fmaxf(__uint_as_float(s[0]), __uint_as_float(s[1]));
  auto t = __builtin_amdgcn_permlane32_swap(__float_as_uint(x), __float_as_uint(x), false, false);
  return fmaxf(__uint_as_float(t[0]), __uint_as_float(t[1]));
}
__device__ __forceinline__ float xq_sum(float x) {
  auto s = __builtin_amdgcn_permlane16_swap(__float_as_uint(x), __float_as_uint(x), false, false);
  x = __uint_as_float(s[0]) + __uint_as_float(s[1]);
  auto t = __builtin_amdgcn_permlane32_swap(__float_as_uint(x), __float_as_uint(x), false, false);
  return __uint_as_float(t[0]) + __uint_as_float(t[1]);
}
__host__ __device__ constexpr int smf_col(int qb, int j) {
  return 32 * (j >> 3) + 16 * (qb >> 1) + 8 * (((j & 7) >> 1) & 1) + 4 * (qb & 1) + 2 * ((j & 7) >> 2) + (j & 1);
}

#define STAMP(slot_, i_) do {} while (0)
#define RD(role_, s_, i_, v_) do {} while (0)
#define RCY(x) do {} while (0)
struct Ctx { rsrc_t rc; gu32* abortw; bool dead; unsigned slot; int seg; };
__device__ __forceinline__ bool flags_ready(const Ctx& c, unsigned vo, unsigned need) {
  const unsigned v = ld4(c.rc, vo, 0);
  asm volatile("" ::: "memory");
  return !__any(v < need);
}
__device__ __forceinline__ void flags_wait(Ctx& c, unsigned vo, unsigned need) {
#pragma unroll 1
  for (unsigned spins = 0; !c.dead; ++spins) {
    if (flags_ready(c, vo, need)) return;
    __builtin_amdgcn_s_sleep(4);
    if ((spins & 63u) == 63u) {
      if (spins > 40000u) __hip_atomic_store(c.abortw, 1u, RLX_AGENT);
      if (__builtin_amdgcn_readfirstlane(__hip_atomic_load(c.abortw, RLX_AGENT))) c.dead = true;
    }
  }
}
template <int BASE>
__device__ __forceinline__ void seg_wait(Ctx& c, int s, int& known, int lane) {
#pragma unroll 1
  for (unsigned spins = 0; known <= s && !c.dead; ++spins) {
    const unsigned v = ld4(c.rc, (BASE + lane * 4) * 4, 0);
    asm volatile("" ::: "memory");
    const unsigned long long miss = __ballot(v == 0u);
    known = miss ? __builtin_ctzll(miss) : 64;
    if (known > s) return;
    __builtin_amdgcn_s_sleep(4);
    if ((spins & 63u) == 63u) {
      if (spins > 40000u) __hip_atomic_store(c.abortw, 1u, RLX_AGENT);
      if (__builtin_amdgcn_readfirstlane(__hip_atomic_load(c.abortw, RLX_AGENT))) c.dead = true;
    }
  }
}
__device__ __forceinline__ unsigned ld4_async(const void* p) {
  unsigned v;
  asm volatile("global_load_dword %0, %1, off sc1" : "=v"(v) : "v"(p) : "memory");
  return v;
}
#define PIN(x) asm volatile("" : "+v"(x))
__device__ __forceinline__ void flag_set(const Ctx& c, unsigned word, unsigned val) { st4(val, c.rc, word * 4u, 0); }

constexpr int PRE_BT = 256, PRE_MOD = 60, PRE_CAL = 16, PRE_GRID = PRE_BT + PRE_MOD + PRE_CAL;

__global__ void __launch_bounds__(256, 1) k_pre(Args a) {
  __shared__ __attribute__((aligned(16))) char sm[32768];
  const unsigned L0 = (unsigned)(size_t)(LAS char*)sm;
  const int tid = threadIdx.x, lane = tid & 63, w = __builtin_amdgcn_readfirstlane(tid >> 6), b = blockIdx.x;
  if (b < PRE_BT) {
    const int h = b >> 3, part = b & 7;
    for (int i = tid; i < 192 * 10; i += 256) {
      const int e = i / 10, ch = i % 10;
      ldsst<v4u>(L0 + e * 160 + ch * 16, *(const v4u*)(a.Wo + ((size_t)h * DH + e) * DM + part * 80 + ch * 8));
    }
    for (int i = b * 256 + tid; i < NSEG * 4096; i += PRE_BT * 256) {
      const unsigned s = (unsigned)i >> 12, byte = ((unsigned)i & 4095u) * 16;
      v4u v = {0xFFFFFFFFu, 0xFFFFFFFFu, 0xFFFFFFFFu, 0xFFFFFFFFu};
      const unsigned o = ((byte >> 14) << 13) + (byte & 8191u);
      if (s == 0 && o < 10240) {
        v = *(const v4u*)(a.mem0 + ((o >> 4) & 7) * DM + (o >> 7) * 8);
        for (int k = 0; k < 4; ++k) v[k] = min(v[k], 0xFFFFFFFEu);
      }
      *(v4u*)(a.ws + O_XR + (size_t)s * XSTEP + byte) = v;
    }
    if (b * 256 + tid < C_WORDS) ((unsigned*)(a.ws + O_CTL))[b * 256 + tid] = 0u;
    __syncthreads();
    v4u bw[6][5];
#pragma unroll
    for (int ks = 0; ks < 6; ++ks)
#pragma unroll
      for (int nt = 0; nt < 5; ++nt) {
        const unsigned ad = L0 + (32 * ks + 8 * (lane >> 4) + ((lane & 15) >> 2)) * 160 + nt * 32 + (lane & 3) * 8;
        bw[ks][nt] = cat(trrd(ad), trrd(ad + 4 * 160));
      }
    for (int T = w; T < 10; T += 4) {
      f32x4 acc[4][5];
#pragma unroll
      for (int tp = 0; tp < 4; ++tp) {
        const int k = 64 * T + smf_col((lane & 15) >> 2, 4 * tp + (lane & 3));
        const u16* ap = a.Wv + (size_t)k * HD + h * DH + 8 * (lane >> 4);
        v4u af[6];
#pragma unroll
        for (int ks = 0; ks < 6; ++ks) af[ks] = *(const v4u*)(ap + 32 * ks);
#pragma unroll
        for (int nt = 0; nt < 5; ++nt) {
          acc[tp][nt] = (f32x4){0.f, 0.f, 0.f, 0.f};
#pragma unroll
          for (int ks = 0; ks < 6; ++ks) acc[tp][nt] = mf32(af[ks], bw[ks][nt], acc[tp][nt]);
        }
      }
#pragma unroll
      for (int nt = 0; nt < 5; ++nt) {
        const int cg = part * 10 + 2 * nt + ((lane & 15) >> 3);
        char* dst = a.ws + O_BT + ((size_t)((cg * 4 + (h >> 3)) * 40 + T * 4 + ((h >> 1) & 3))) * 2048 + ((lane >> 4) * 16 + (h & 1) * 8 + (lane & 7)) * 32;
        *(v4u*)dst = cat(pack4(acc[0][nt]), pack4(acc[1][nt]));
        *(v4u*)(dst + 16) = cat(pack4(acc[2][nt]), pack4(acc[3][nt]));
      }
    }
  } else if (b < PRE_BT + PRE_MOD) {
    const int n0 = (b - PRE_BT) * 64 + (tid & 7) * 8, k0 = (tid >> 3) * 20;
    float acc[8] = {0.f, 0.f, 0.f, 0.f, 0.f, 0.f, 0.f, 0.f};
    v4u wv[20];
#pragma unroll
    for (int k = 0; k < 20; ++k) wv[k] = *(const v4u*)(a.wmod + (size_t)(k0 + k) * (6 * DM) + n0);
#pragma unroll
    for (int k = 0; k < 20; ++k) {
      const float cv = bf2f(a.c[k0 + k]);
      const float sc = rbf(cv / (1.f + __expf(-cv)));
#pragma unroll
      for (int j = 0; j < 4; ++j) { acc[2 * j] += sc * blo(wv[k][j]); acc[2 * j + 1] += sc * bhi(wv[k][j]); }
    }
#pragma unroll
    for (int j = 0; j < 8; ++j) ldsst<float>(L0 + ((tid >> 3) * 64 + (tid & 7) * 8 + j) * 4, acc[j]);
    __syncthreads();
    if (tid < 64) {
      float s = 0.f;
      for (int k = 0; k < 32; ++k) s += ldsld<float>(L0 + (k * 64 + tid) * 4);
      const int n = (b - PRE_BT) * 64 + tid, which = n / DM;
      const float v = rbf(s);
      ((float*)(a.ws + O_MOD))[n] = v;
      if (which != 2 && which != 5) {
        const bool isc = which == 1 || which == 4;
        ((u16*)(a.ws + O_MODP))[((which >= 3 ? 2 : 0) + (isc ? 0 : 1)) * DM + n % DM] = (u16)(cvtpk(isc ? 1.f + v : v, 0.f) & 0xFFFFu);
      }
    }
  } else {
    const int s = (b - PRE_BT - PRE_MOD) * 4 + w;
    const rsrc_t r = mkrs(a.ws + O_XR, (size_t)NSEG * XSTEP);
    unsigned best[2] = {0xFFFFFFFFu, 0xFFFFFFFFu};
    for (int p = 0; p < 4; ++p)
      for (int hf = 0; hf < 2; ++hf) {
        const unsigned off = s * XSTEP + 65536u + hf * 8192u + p * 2048u;
        st4(0u, r, off, 0);
        VMWAIT(0);
        const unsigned t0 = cy32();
        asm volatile("s_waitcnt lgkmcnt(0)" ::: "memory");
        const unsigned v = ld4(r, off, 0);
        asm volatile("s_waitcnt vmcnt(0)" :: "v"(v) : "memory");
        best[hf] = min(best[hf], cy32() - t0);
      }
    unsigned nearh = best[1] < best[0] ? 1u : 0u;
    if (xcc_id() >= 4) nearh ^= 1u;
    if (lane == 0) ((unsigned*)(a.ws + O_SEL))[s] = nearh;
  }
}

template <bool COH>
__device__ __forceinline__ void ln_segment(const char* src, const char* gs, rsrc_t rdst, unsigned dbase, int w, int lane) {
  const rsrc_t rs = mkrs(src, 32 * DM * 2);
  const int c16 = lane & 15, row0 = w * 8 + (lane >> 4);
  v4u v[2][5], g[5], sh[5];
#pragma unroll
  for (int p = 0; p < 2; ++p)
#pragma unroll
    for (int i = 0; i < 5; ++i) v[p][i] = __builtin_amdgcn_raw_buffer_load_b128(rs, (row0 + 4 * p) * (DM * 2) + (c16 + 16 * i) * 16, 0, COH ? 16 : 0);
#pragma unroll
  for (int i = 0; i < 5; ++i) { g[i] = *(const v4u*)(gs + (c16 + 16 * i) * 16); sh[i] = *(const v4u*)(gs + DM * 2 + (c16 + 16 * i) * 16); }
#pragma unroll
  for (int p = 0; p < 2; ++p) {
    f32x2 f[20];
    f32x2 s2 = {0.f, 0.f};
#pragma unroll
    for (int i = 0; i < 20; ++i) { f[i] = (f32x2){blo(v[p][i >> 2][i & 3]), bhi(v[p][i >> 2][i & 3])}; s2 += f[i]; }
    const float mean = row16_sum(s2.x + s2.y) * (1.f / DM);
    f32x2 q2 = {0.f, 0.f};
#pragma unroll
    for (int i = 0; i < 20; ++i) { const f32x2 d = f[i] - mean; q2 += d * d; }
    const float m = rbf(mean), var = rbf(row16_sum(q2.x + q2.y) * (1.f / DM));
    const float inv = 1.f / rbf(sqrtf(rbf(var + 1e-5f)));
    const int row = row0 + 4 * p;
#pragma unroll
    for (int i = 0; i < 5; ++i) {
      v4u o;
#pragma unroll
      for (int j = 0; j < 4; ++j) {
        const f32x2 dd = f[4 * i + j] - m;
        const unsigned d = cvtpk(dd.x, dd.y);
        const f32x2 nn = (f32x2){blo(d), bhi(d)} * inv;
        const unsigned n = cvtpk(nn.x, nn.y);
        const f32x2 tt = (f32x2){blo(n), bhi(n)} * (f32x2){blo(g[i][j]), bhi(g[i][j])};
        const unsigned t = cvtpk(tt.x, tt.y);
        const f32x2 oo = (f32x2){blo(t), bhi(t)} + (f32x2){blo(sh[i][j]), bhi(sh[i][j])};
        o[j] = cvtpk(oo.x, oo.y);
      }
      const int ch = c16 + 16 * i;
      st16(o, rdst, ((row >> 4) * 20 + (ch >> 2)) * 1024 + ((ch & 3) * 16 + (row & 15)) * 16, dbase);
    }
  }
}

struct Sched {
  unsigned g1, g2, e1, e2; int r1, r2, w1, w2, c1, c2; bool v1, v2;
  __device__ __forceinline__ void init(int W1, int W2) { g1 = g2 = e1 = e2 = 0; r1 = r2 = -1; w1 = W1; w2 = W2; c1 = c2 = 0; v1 = v2 = false; }
  static __device__ __forceinline__ int upd(int r, int x, int c) {
    x = min(max(x, 4), 1200);
    const int f = x > r ? min(x, r + 24) : (r + x) >> 1;
    return c == 0 ? r : (r < 0 ? x : (c == 2 ? f : r));
  }
  __device__ __forceinline__ void got_b(unsigned b) {
    const bool ok = v2 && (int)(g2 - b) >= w2 - 12;
    e2 = ok ? g2 : b + w2 + 48; c2 = ok ? 2 : 1;
    r1 = upd(r1, (int)(b - e1), c1);
  }
  __device__ __forceinline__ void got_a(unsigned a) {
    const bool ok = v1 && (int)(g1 - a) >= w1 - 12;
    e1 = ok ? g1 : a + w1 + 48; c1 = ok ? 2 : 1;
    r2 = upd(r2, (int)(a - e2), c2);
    v2 = r1 >= 0; g2 = e1 + r1 + w2;
    v1 = v2 && r2 >= 0; g1 = g2 + r2 + w1;
  }
};
struct Clock {
  unsigned rt0, cy0, rte, cye; float upc, cpu;
  static __device__ __forceinline__ void edge(unsigned& r, unsigned& c) {
    const unsigned r0 = rt32();
#pragma unroll 1
    for (int k = 0; k < 64; ++k) { r = rt32(); c = cy32(); if (r != r0) break; }
  }
  __device__ __forceinline__ void init() { edge(rt0, cy0); rte = rt0; cye = cy0; cpu = 5.9f; upc = 1.f / 5.9f; }
  __device__ __forceinline__ void sync() {
    edge(rte, cye);
    const float dr = (float)(rte - rt0) * 4.f, dc = (float)(cye - cy0);
    cpu = __builtin_bit_cast(float, __builtin_amdgcn_readfirstlane(__builtin_bit_cast(int, dc * __builtin_amdgcn_rcpf(dr))));
    upc = __builtin_bit_cast(float, __builtin_amdgcn_readfirstlane(__builtin_bit_cast(int, dr * __builtin_amdgcn_rcpf(dc))));
  }
  __device__ __forceinline__ unsigned time_of(unsigned c) const { return rte * 4u + (unsigned)__builtin_amdgcn_readfirstlane((int)((float)(int)(c - cye) * upc)); }
  __device__ __forceinline__ unsigned cycle_of(unsigned t) const { return cye + (unsigned)__builtin_amdgcn_readfirstlane((int)((float)(int)(t - rte * 4u) * cpu)); }
};
#define DPPM " row_mask:0xf bank_mask:0xf\n\t"
__device__ __forceinline__ int wave_max_i32(int v) {
  int x, y;
  asm("s_nop 1\n\t"
      "v_max_i32_dpp %0, %2, %2 quad_perm:[1,0,3,2]" DPPM "s_nop 1\n\t"
      "v_max_i32_dpp %0, %0, %0 quad_perm:[2,3,0,1]" DPPM "s_nop 1\n\t"
      "v_max_i32_dpp %0, %0, %0 row_half_mirror" DPPM "s_nop 1\n\t"
      "v_max_i32_dpp %1, %0, %0 row_mirror" DPPM
      "v_max_i32_dpp %0, %0, %0 row_mirror" DPPM "s_nop 1\n\t"
      "v_permlane16_swap_b32 %0, %1\n\ts_nop 0\n\t"
      "v_max_i32 %0, %0, %1\n\t"
      "v_mov_b32 %1, %0\n\ts_nop 1\n\t"
      "v_permlane32_swap_b32 %0, %1\n\ts_nop 0\n\t"
      "v_max_i32 %0, %0, %1\n\ts_nop 0" : "=&v"(x), "=&v"(y) : "v"(v));
  return __builtin_amdgcn_readfirstlane(x);
}
__device__ __forceinline__ float red32_max(float a, float b) {
  float x, y;
  asm("v_max_f32 %0, %2, %3\n\ts_nop 1\n\t"
      "v_max_f32_dpp %0, %0, %0 quad_perm:[1,0,3,2]" DPPM "s_nop 1\n\t"
      "v_max_f32_dpp %0, %0, %0 quad_perm:[2,3,0,1]" DPPM "s_nop 1\n\t"
      "v_max_f32_dpp %0, %0, %0 row_half_mirror" DPPM "s_nop 1\n\t"
      "v_max_f32_dpp %1, %0, %0 row_mirror" DPPM
      "v_max_f32_dpp %0, %0, %0 row_mirror" DPPM "s_nop 1\n\t"
      "v_permlane16_swap_b32 %0, %1\n\ts_nop 0\n\t"
      "v_max_f32 %0, %0, %1" : "=&v"(x), "=&v"(y) : "v"(a), "v"(b));
  return x;
}
__device__ __forceinline__ float red32_sum(float a, float b) {
  float x, y;
  asm("v_add_f32 %0, %2, %3\n\ts_nop 1\n\t"
      "v_add_f32_dpp %0, %0, %0 quad_perm:[1,0,3,2]" DPPM "s_nop 1\n\t"
      "v_add_f32_dpp %0, %0, %0 quad_perm:[2,3,0,1]" DPPM "s_nop 1\n\t"
      "v_add_f32_dpp %0, %0, %0 row_half_mirror" DPPM "s_nop 1\n\t"
      "v_add_f32_dpp %1, %0, %0 row_mirror" DPPM
      "v_add_f32_dpp %0, %0, %0 row_mirror" DPPM "s_nop 1\n\t"
      "v_permlane16_swap_b32 %0, %1\n\ts_nop 0\n\t"
      "v_add_f32 %0, %0, %1" : "=&v"(x), "=&v"(y) : "v"(a), "v"(b));
  return x;
}
struct Stamps { unsigned t0, t1; };
__device__ __forceinline__ Stamps stamps_load(rsrc_t rx, unsigned o0, unsigned o1, unsigned sbase) { return Stamps{ld4(rx, o0, sbase), ld4(rx, o1, sbase)}; }
__device__ __forceinline__ unsigned stamps_latest(Stamps s, rsrc_t rx, unsigned o0, unsigned o1, unsigned sbase, unsigned ref, bool dead) {
#pragma unroll 1
  for (int k = 0; k < 4000; ++k) {
    if (!__any(s.t0 == 0xFFFFFFFFu || s.t1 == 0xFFFFFFFFu) || dead) break;
    __builtin_amdgcn_s_sleep(1);
    asm volatile("" ::: "memory");
    s = stamps_load(rx, o0, o1, sbase);
  }
  return ref + (unsigned)wave_max_i32(max((int)(s.t0 - ref), (int)(s.t1 - ref)));
}
template <int NB>
__device__ __forceinline__ void poll(rsrc_t r, const unsigned (&voff)[NB], unsigned soff, v4u (&v)[NB], Ctx& c) {
#pragma unroll 1
  for (unsigned spins = 0;; ++spins) {
#pragma unroll
    for (int k = 0; k < NB; ++k) v[k] = ld16(r, voff[k], soff);
    unsigned m = 0;
#pragma unroll
    for (int k = 0; k < NB; ++k) m = max(m, umax4(v[k]));
    asm volatile("" ::: "memory");
    if (!__any(m == 0xFFFFFFFFu) || c.dead) return;
    if ((spins & 255u) == 255u) {
      if (spins > 60000u) __hip_atomic_store(c.abortw, 1u, RLX_AGENT);
      if (__builtin_amdgcn_readfirstlane(__hip_atomic_load(c.abortw, RLX_AGENT))) c.dead = true;
    }
  }
}
__device__ __forceinline__ v2u fold_pack(f32x4 a, f32x4 b) {
  float f[4];
#pragma unroll
  for (int r = 0; r < 4; ++r) {
    auto sw = __builtin_amdgcn_permlane32_swap(__float_as_uint(a[r]), __float_as_uint(b[r]), false, false);
    f[r] = __uint_as_float(sw[0]) + __uint_as_float(sw[1]);
  }
  return (v2u){cvtpk(f[0], f[1]), cvtpk(f[2], f[3])};
}

__device__ __forceinline__ void stage384(const char* W, size_t ldb, int row0, int n3, unsigned dst, int w, int lane) {
#pragma unroll 1
  for (int i = w; i < 3 * n3; i += 4) {
    const unsigned byte = (i % 3) * 1024 + lane * 16, row = byte / 384, col = byte % 384;
    dma16p(W + (size_t)(row0 + 8 * (i / 3) + row) * ldb + col, dst + i * 1024);
  }
}

constexpr int L1_IMG = 0, L1_PART = 10240, L1_W = L1_PART + 6144, MAIN_LDS = L1_W + 4 * 18 * 2048;
static_assert(MAIN_LDS == 163840, "lds");

template <int W> __device__ constexpr int l1_slot(int T) { return T >= 8 ? T : ((T - W) & 3) == 0 ? (T >> 2) : (T < 4 ? 1 : 4) + ((T - W) & 3); }
template <int W>
__device__ __forceinline__ void l1_weights(const Args& a, const int h, const unsigned L0, const int lane, v8u (&wq)[3][10], v8u (&wk)[3][4]) {
  const unsigned wl = L0 + L1_W + W * 18 * 2048 + lane * 16;
  const unsigned trb = (16 * (lane >> 5) + 4 * ((lane >> 4) & 1) + ((lane & 15) >> 2)) * 384 + (3 * W) * 32 + (lane & 3) * 8;
  auto frag = [&](unsigned base, int t) {
    const unsigned ad = base + trb + t * 32;
    const v2u r0 = trrd(ad), r1 = trrd(ad + 8 * 384), r2 = trrd(ad + 32 * 384), r3 = trrd(ad + 40 * 384);
    return (v8u){r0.x, r1.x, r0.y, r1.y, r2.x, r3.x, r2.y, r3.y};
  };
  auto stage = [&](int r) { stage384((const char*)((r < 5 ? a.Wq : a.Wk) + h * DH), HD * 2, 128 * (r % 5), 16, L0 + (r & 1) * 49152, W, lane); };
  v8u tmp[6][3];
  stage(0); stage(1);
#pragma unroll
  for (int r = 0; r < 10; ++r) {
    if (r < 9) VMWAIT(12); else VMWAIT(0);
    barx();
#pragma unroll
    for (int k = 0; k < 2; ++k)
#pragma unroll
      for (int t = 0; t < 3; ++t) {
        const int T = 2 * (r % 5) + k, sl = l1_slot<W>(T);
        const v8u f = frag(L0 + (r & 1) * 49152 + k * 24576, t);
        if (r < 5) wq[t][sl] = f; else if (T < 4) wk[t][sl == 0 ? 0 : (sl - 1) & 3] = f; else tmp[T < 4 ? 0 : T - 4][t] = f;
      }
    barx();
    if (r < 8) stage(r + 2);
  }
#pragma unroll
  for (int T = 4; T < 10; ++T)
#pragma unroll
    for (int t = 0; t < 3; ++t) {
      const int sl = l1_slot<W>(T), n = sl == 1 ? t : 3 + t * 5 + (sl - 5);
      const v8u f = tmp[T - 4][t];
      ldsst<v4u>(wl + n * 2048, __builtin_shufflevector(f, f, 0, 1, 2, 3));
      ldsst<v4u>(wl + n * 2048 + 1024, __builtin_shufflevector(f, f, 4, 5, 6, 7));
    }
}

__device__ __forceinline__ void layer1(const Args& a, const int h, const unsigned L0, Ctx& cx) {
  const int tid = threadIdx.x, lane = tid & 63, w = __builtin_amdgcn_readfirstlane(tid >> 6);
  const rsrc_t rx = mkrs(a.ws + O_XR, (size_t)NSEG * XSTEP), rk = mkrs(a.ws + O_KX, (size_t)S * HD * 2), rkm = mkrs(a.ws + O_KM, (size_t)NSEG * H * 6144);
  const unsigned wl = L0 + L1_W + w * 18 * 2048 + lane * 16;
  v8u wq[3][10], wk[3][4];
  if (w == 0) l1_weights<0>(a, h, L0, lane, wq, wk); else if (w == 1) l1_weights<1>(a, h, L0, lane, wq, wk);
  else if (w == 2) l1_weights<2>(a, h, L0, lane, wq, wk); else l1_weights<3>(a, h, L0, lane, wq, wk);
  const unsigned long long selmask = __ballot(((const unsigned*)(a.ws + O_SEL))[lane] != 0u);
  const unsigned sel_lo = __builtin_amdgcn_readfirstlane((unsigned)selmask), sel_hi = __builtin_amdgcn_readfirstlane((unsigned)(selmask >> 32));
  const int sidx = (lane & 8) ? 0xEEEE : 0x4444;
  unsigned moff[3];
#pragma unroll
  for (int k = 0; k < 3; ++k) { int b = w + 4 * k; if (b >= 10) b = w; moff[k] = xmap(b * 1024 + lane * 16); }
  const unsigned img = L0 + L1_IMG + lane * 16, mst2 = img + (w < 2 ? w + 8 : w) * 1024;
  unsigned aad[3];
#pragma unroll
  for (int i = 0; i < 3; ++i) aad[i] = img + ((w + 1 + i) & 3) * 1024;
  const int qi = 2 * w + (lane >> 5), j = lane & 31;
  const unsigned poff = xmap(XP + (w >> 1) * 10240 + (w & 1) * 128 + (lane >> 5) * 16 +
                             ((j & 1) ? (8 + (h >> 4)) * 1024 + ((h >> 2) & 3) * 256 + (h & 3) * 32 + (j - 1) * 2
                                      : (h >> 2) * 1024 + (h & 3) * 256 + ((j & 15) >> 2) * 32 + ((j >> 4) * 4 + (j & 3)) * 2));
  const bool pstore = !(j & 1) || j < 8;
  const unsigned t1o = xmap(XT1 + (lane & 31) * 16), t2o0 = xmap(XT2 + lane * 16), t2o1 = xmap(XT2 + 1024 + (lane & 15) * 16);
  const int dq = (lane & 15) - 4 * (lane >> 4);
  const v4u ident = {dq == 0 ? 0x3F80u : dq == 1 ? 0x3F800000u : 0u, dq == 2 ? 0x3F80u : dq == 3 ? 0x3F800000u : 0u, 0u, 0u};
  const unsigned kxo = (h * 4 + w) * 3072 + lane * 16, kfo = (C_KF + h * 32) * 4;
  STAMP(cx.slot, 1);
  v4u kx[3];
  flags_wait(cx, kfo, 1u);
#pragma unroll
  for (int t = 0; t < 3; ++t) kx[t] = ld16(rk, kxo + t * 1024, 0);
  unsigned kready = __builtin_amdgcn_readfirstlane(ld4(cx.rc, kfo, 0));
  Sched sc; sc.init(a.w1q, a.w2q);
  Clock ck; ck.init();
  unsigned gate = cy32(), pbase = 0;
  const f32x4 Z4 = {0.f, 0.f, 0.f, 0.f};
#pragma unroll 1
  for (int s = 0; s < NSEG; ++s) {
    const unsigned sbase = s * XSTEP + (((s < 32 ? sel_lo : sel_hi) >> (s & 31)) & 1u) * 8192u;
    f32x4 aq[3], ak[3];
#pragma unroll
    for (int t = 0; t < 3; ++t) { aq[t] = Z4; ak[t] = Z4; asm volatile("" : "+v"(aq[t]), "+v"(ak[t])); }
    v4u llo[3], lhi[3];
#pragma unroll
    for (int i = 0; i < 3; ++i) { llo[i] = ldsld<v4u>(wl + i * 2048); lhi[i] = ldsld<v4u>(wl + i * 2048 + 1024); asm volatile("" : "+v"(llo[i]), "+v"(lhi[i])); }
    SB();
    RCY(d_rdy);
    wait_cy(gate);
    v4u mv[3];
    poll<3>(rx, moff, sbase, mv, cx);
    RCY(d_det);
    ldsst<v4u>(img + w * 1024, mv[0]);
    ldsst<v4u>(img + (w + 4) * 1024, mv[1]);
    ldsst<v4u>(mst2, mv[2]);
    SB();
#pragma unroll
    for (int t = 0; t < 3; ++t) { aq[t] = smf(mv[0], wq[t][0], aq[t], sidx); ak[t] = smf(mv[0], wk[t][0], ak[t], sidx); }
    SB();
    barx();
    v4u am[8];
#pragma unroll
    for (int i = 0; i < 8; ++i) am[i] = ldsld<v4u>(i < 6 ? aad[i % 3] + (i / 3) * 4096 : img + (i + 2) * 1024);
    SB();
#pragma unroll
    for (int t = 0; t < 3; ++t) {
      aq[t] = smf(mv[1], wq[t][1], aq[t], sidx);
      ak[t] = smf(mv[1], cat8(llo[t], lhi[t]), ak[t], sidx);
      SB();
    }
    if (tid == 192) flag_set(cx, C_MF1 + h * 32, 4u * s);
    SB();
    f32x4 s0 = Z4, s1 = Z4, sm = Z4, xt;
    v4u X01;
    v2u xa;
    unsigned sw[8];
    float ff[4];
#pragma unroll
    for (int t = 0; t < 3; ++t) {
#pragma unroll
      for (int i = 0; i < 8; ++i) {
        const int n = 3 + t * 5 + (i - 3);
        aq[t] = smf(am[i], wq[t][2 + i], aq[t], sidx);
        SB();
        if (t == 0 && i < 3) llo[i] = ldsld<v4u>(wl + (3 + i) * 2048);
        if (i >= 4 && n + 2 < 18) lhi[(n - 1) % 3] = ldsld<v4u>(wl + (n + 2) * 2048 + 1024);
        if (i == 0 && t > 0 && t * 5 + 5 < 18) lhi[(t * 5 + 2) % 3] = ldsld<v4u>(wl + (t * 5 + 5) * 2048 + 1024);
        if (t > 0) {
          if (i == 1) { for (int r = 0; r < 2; ++r) { auto x = __builtin_amdgcn_permlane32_swap(__float_as_uint(aq[t - 1][r]), __float_as_uint(ak[t - 1][r]), false, false); sw[2 * r] = x[0]; sw[2 * r + 1] = x[1]; }
                        ff[0] = __uint_as_float(sw[0]) + __uint_as_float(sw[1]); }
          if (i == 2) { ff[2] = __uint_as_float(sw[4]) + __uint_as_float(sw[5]); ff[3] = __uint_as_float(sw[6]) + __uint_as_float(sw[7]); }
          if (i == 3) xt = mf32((v4u){xa.x, xa.y, 0u, 0u}, ident, Z4);
          if (i == 5) X01[2 * (t - 1) + 1] = cvtpk(xt[2], xt[3]);
          if (t == 2 && i == 6) s0 = mf32(kx[0], X01, s0);
          if (t == 2 && i == 7) sm = mf32(X01, X01, sm);
        }
        SB();
        ak[t] = smf(am[i], i < 3 ? wk[t][i < 3 ? 1 + i : 0] : cat8(llo[(n + 18) % 3], lhi[(n + 18) % 3]), ak[t], sidx);
        SB();
        if (t == 0 && i < 3) lhi[i] = ldsld<v4u>(wl + (3 + i) * 2048 + 1024);
        if (i >= 3 && n + 3 < 18) llo[n % 3] = ldsld<v4u>(wl + (n + 3) * 2048);
        if (t > 0) {
          if (i == 1) { for (int r = 2; r < 4; ++r) { auto x = __builtin_amdgcn_permlane32_swap(__float_as_uint(aq[t - 1][r]), __float_as_uint(ak[t - 1][r]), false, false); sw[2 * r] = x[0]; sw[2 * r + 1] = x[1]; }
                        ff[1] = __uint_as_float(sw[2]) + __uint_as_float(sw[3]); }
          if (i == 2) { xa.x = cvtpk(ff[0], ff[1]); xa.y = cvtpk(ff[2], ff[3]); }
          if (i == 4) X01[2 * (t - 1)] = cvtpk(xt[0], xt[1]);
          if (t == 2 && i == 6) s1 = mf32(kx[1], X01, s1);
        }
        SB();
      }
    }
    xa = fold_pack(aq[2], ak[2]);
    xt = mf32((v4u){xa.x, xa.y, 0u, 0u}, ident, Z4);
    const v2u X2 = pack4(xt);
    unsigned cy_pub = 0, t_pub = 0;
    if (s < NSEG - 1) {
      const v4u xz = {X2.x, X2.y, 0u, 0u};
      s0 = mf32(kx[2], xz, s0);
      s1 = mf32(kx[2], (v4u){0u, 0u, X2.x, X2.y}, s1);
      sm = mf32(xz, xz, sm);
      if ((lane & 8) == 0) {
        const unsigned pa = L0 + L1_PART + w * 1536 + (lane & 7) * 64 + (lane >> 4) * 16;
        ldsst<f32x4>(pa, s0); ldsst<f32x4>(pa + 512, s1); ldsst<f32x4>(pa + 1024, sm);
      }
      barx();
      const unsigned ra = L0 + L1_PART + (j >> 4) * 512 + qi * 64 + (j & 15) * 4;
      const unsigned rb = L0 + L1_PART + 1024 + qi * 64 + (8 + (j & 7)) * 4;
      const float x0 = ldsld<float>(ra), x1 = ldsld<float>(ra + 1536), x2 = ldsld<float>(ra + 3072), x3 = ldsld<float>(ra + 4608);
      const float y0 = ldsld<float>(rb), y1 = ldsld<float>(rb + 1536), y2 = ldsld<float>(rb + 3072), y3 = ldsld<float>(rb + 4608);
      SB();
      const unsigned r1 = cvtpk((x0 + x1) + (x2 + x3), (y0 + y1) + (y2 + y3));
      const unsigned r2 = cvtpk(blo(r1) * SCALE, bhi(r1) * SCALE);
      const float sx = blo(r2), sy = bhi(r2);
      const float mx = red32_max(sx, sy);
      const float ex = __builtin_amdgcn_exp2f((sx - mx) * LOG2E), ey = __builtin_amdgcn_exp2f((sy - mx) * LOG2E);
      const float inv = __builtin_amdgcn_rcpf(red32_sum(ex, j < 8 ? ey : 0.f));
      const float own = ((j & 1) ? ey : ex) * inv, snd = ((j & 1) ? ex : ey) * inv;
      const float oth = dppf<0xB1>(snd);
      const unsigned pv = min((j & 1) ? cvtpk(oth, own) : cvtpk(own, oth), 0xFFFFFFFEu);
      if (pstore) st4(pv, rx, poff, sbase);
      SB();
      cy_pub = cy32();
      t_pub = ck.time_of(cy_pub);
      if (tid == 0) st4(min(t_pub, 0xFFFFFFFEu), rx, xmap(XT1 + h * 16), sbase);
    }
    const Stamps st2 = stamps_load(rx, t2o0, t2o1, sbase);
    const unsigned t1v = ld4(rx, t1o, pbase), kfv = ld4(cx.rc, kfo, 0);
    const bool kpre = s < NSEG - 2 && kready >= (unsigned)s + 2u;
    if (kpre) {
#pragma unroll
      for (int t = 0; t < 3; ++t) kx[t] = ld16(rk, kxo + t * 1024, (s + 1) * (H * 12288));
    }
    st16(X01, rkm, (h * 4 + w) * 1536 + lane * 16, s * (H * 6144));
    st8(X2, rkm, (h * 4 + w) * 1536 + 1024 + lane * 8, s * (H * 6144));
    SB();
    ck.sync();
    kready = __builtin_amdgcn_readfirstlane(kfv);
    if (s < NSEG - 1) {
      if (s > 0) {
        sc.got_b(stamps_latest(Stamps{t1v, t1v}, rx, t1o, t1o, pbase, t_pub, cx.dead));
        sc.got_a(stamps_latest(st2, rx, t2o0, t2o1, sbase, t_pub, cx.dead));
      }
      gate = sc.v1 ? ck.cycle_of(sc.g1) : cy_pub;
      if (!kpre && s < NSEG - 2) {
        if (kready < (unsigned)s + 2u) flags_wait(cx, kfo, s + 2);
#pragma unroll
        for (int t = 0; t < 3; ++t) kx[t] = ld16(rk, kxo + t * 1024, (s + 1) * (H * 12288));
      }
    }
    RCY(d_end); RD(h, s, 1, d_det - d_rdy); RD(h, s, 2, cy_pub - d_det); RD(h, s, 3, sc.r1); RD(h, s, 4, sc.r2); RD(h, s, 5, d_end - cy_pub); RD(h, s, 6, gate - cy_pub);
    pbase = sbase;
  }
  VMWAIT(0); barx();
  if (tid == 192) flag_set(cx, C_MF1 + h * 32, 4u * NSEG);
}

constexpr int L2_IMG = 0, L2_UM = 10240, L2_UX = L2_UM + 4096, L2_W = L2_UX + 2 * 16384, L2_MISC = L2_W + 4 * 8 * 2048;

__device__ __forceinline__ void layer2(const Args& a, const int cg, const unsigned L0, Ctx& cx) {
  const int tid = threadIdx.x, lane = tid & 63, w = __builtin_amdgcn_readfirstlane(tid >> 6);
  const rsrc_t rx = mkrs(a.ws + O_XR, (size_t)NSEG * XSTEP), rum = mkrs(a.ws + O_UM, (size_t)NSEG * 80 * 4096);
  const unsigned wl = L0 + L2_W + w * 8 * 2048 + lane * 16;
  v8u wr[8][4];
  {
    const v4u* src = (const v4u*)(a.ws + O_BT + ((size_t)(cg * 4 + w) * 40) * 2048) + lane * 2;
#pragma unroll
    for (int f = 0; f < 40; ++f) {
      const v4u lo = src[f * 128], hi = src[f * 128 + 1];
      if (f >= 32) { ldsst<v4u>(wl + (f - 32) * 2048, lo); ldsst<v4u>(wl + (f - 32) * 2048 + 1024, hi); }
      else wr[f >> 2][f & 3] = cat8(lo, hi);
    }
  }
  const unsigned long long selmask = __ballot(((const unsigned*)(a.ws + O_SEL))[lane] != 0u);
  const unsigned sel_lo = __builtin_amdgcn_readfirstlane((unsigned)selmask), sel_hi = __builtin_amdgcn_readfirstlane((unsigned)(selmask >> 32));
  const int sidx = (lane & 8) ? 0xEEEE : 0x4444;
  unsigned moff[4], po[10];
#pragma unroll
  for (int k = 0; k < 4; ++k) { int b = w + 3 * k; if (b >= 10) b = w; moff[k] = xmap(b * 1024 + lane * 16); }
#pragma unroll
  for (int u = 0; u < 10; ++u) po[u] = xmap(XP + (w & 1) * 10240 + u * 1024 + lane * 16);
  const unsigned mst0 = L0 + L2_IMG + w * 1024 + lane * 16, mst3 = L0 + L2_IMG + (w == 0 ? 9 : w) * 1024 + lane * 16;
  const unsigned umo = L0 + L2_UM + ((w >> 1) * 2 + (lane & 1)) * 1024 + ((2 * (w & 1)) * 16 + 4 * ((lane & 7) >> 1) + 2 * (lane >> 5) + ((lane >> 3) & 1)) * 16 + ((lane >> 4) & 1) * 8;
  const unsigned ooff = xmap(cg * 128 + (4 * (w & 1) + 2 * ((lane >> 3) & 1) + (lane & 1)) * 16 + (lane >> 4) * 4);
  const unsigned t1o = xmap(XT1 + (lane & 31) * 16), t2o0 = xmap(XT2 + lane * 16), t2o1 = xmap(XT2 + 1024 + (lane & 15) * 16);
  const unsigned ufo = (C_UF + (lane & 31) * 4) * 4;
  auto ux_dma = [&](int s) {
    const char* up = a.ws + O_UX + ((size_t)s * 80 + cg) * 16384 + lane * 16;
#pragma unroll
    for (int v = 0; v < 16; ++v) dma16(up + v * 1024, L0 + L2_UX + (s & 1) * 16384 + v * 1024);
  };
  STAMP(cx.slot, 1);
  if (w == 3) { flags_wait(cx, ufo, 1u); ux_dma(0); }
  unsigned ufv = 0;
  unsigned tref = cy32();
  Sched sc; sc.init(a.w1q, a.w2q);
  Clock ck; ck.init();
  unsigned pbase = 0;
  const f32x4 Z4 = {0.f, 0.f, 0.f, 0.f};
#pragma unroll 1
  for (int s = 0; s < NSEG; ++s) {
    const unsigned sbase = s * XSTEP + (((s < 32 ? sel_lo : sel_hi) >> (s & 31)) & 1u) * 8192u;
    const unsigned nbase = (s + 1) * XSTEP + (((s + 1 < 32 ? sel_lo : sel_hi) >> ((s + 1) & 31)) & 1u) * 8192u;
    f32x4 acc[4];
#pragma unroll
    for (int t = 0; t < 4; ++t) { acc[t] = Z4; asm volatile("" : "+v"(acc[t])); }
    Stamps st1 = {0u, 0u}, st2 = {0u, 0u};
    if (w == 1 && s > 0) st1 = stamps_load(rx, t1o, t1o, pbase);
    if (w < 2 && s > 0) ck.sync();
    if (w < 3) {
      if (s > 0) wait_cy(tref + a.w1q * 6 + a.l2d);
      v4u mv[4];
      poll<4>(rx, moff, sbase, mv, cx);
      ldsst<v4u>(mst0, mv[0]); ldsst<v4u>(mst0 + 3072, mv[1]); ldsst<v4u>(mst0 + 6144, mv[2]); ldsst<v4u>(mst3, mv[3]);
    }
    barx();
    v4u am[10];
#pragma unroll
    for (int T = 0; T < 10; ++T) am[T] = ldsld<v4u>(L0 + L2_IMG + T * 1024 + lane * 16);
    if (w == 1 && s > 0) st2 = stamps_load(rx, t2o0, t2o1, sbase);
    bool early = false;
    if (w == 3) {
      VMWAIT(0);
      if (s > 0 && lane == 0) flag_set(cx, C_MF2 + cg * 4, s);
      early = s + 1 < NSEG - 1 && !__any(ufv < (unsigned)s + 2u);
      if (early) ux_dma(s + 1);
    }
    SB();
    v4u lw[16];
#pragma unroll
    for (int T = 0; T < 10; ++T)
#pragma unroll
      for (int t = 0; t < 4; ++t) {
        const int i = T * 4 + t;
        acc[t] = smf(am[T], T < 8 ? wr[T < 8 ? T : 0][t] : cat8(lw[2 * (i & 7)], lw[2 * (i & 7) + 1]), acc[t], sidx);
        SB();
        if (i >= 8 && i < 24) { lw[i - 8] = ldsld<v4u>(wl + (i - 8) * 1024); SB(); }
      }
#pragma unroll
    for (int tp = 0; tp < 2; ++tp) ldsst<v2u>(umo + tp * 256, fold_pack(acc[2 * tp], acc[2 * tp + 1]));
    if (w == 1) {
      if (s > 0) {
        const unsigned ref = ck.rte * 4u;
        sc.got_b(stamps_latest(st1, rx, t1o, t1o, pbase, ref, cx.dead));
        sc.got_a(stamps_latest(st2, rx, t2o0, t2o1, sbase, ref, cx.dead));
      }
      ldsst<v2u>(L0 + L2_MISC + 8, (v2u){ck.cycle_of(sc.g2), sc.v2 ? 1u : 0u});
    }
    pbase = sbase;
    barx();
    if (w == 3 && s + 1 < NSEG - 1 && !early) { flags_wait(cx, ufo, s + 2); ux_dma(s + 1); }
    if (s < NSEG - 1) {
      if (w < 2) {
        v4u ua[20];
        const v2u gv = ldsld<v2u>(L0 + L2_MISC + 8);
#pragma unroll
        for (int u = 0; u < 16; ++u) ua[u] = ldsld<v4u>(L0 + L2_UX + (s & 1) * 16384 + u * 1024 + lane * 16);
#pragma unroll
        for (int u = 0; u < 4; ++u) ua[16 + u] = ldsld<v4u>(L0 + L2_UM + u * 1024 + lane * 16);
#pragma unroll
        for (int u = 0; u < 20; u += 4) asm volatile("" : "+v"(ua[u]), "+v"(ua[u + 1]), "+v"(ua[u + 2]), "+v"(ua[u + 3]));
        const unsigned g2 = __builtin_amdgcn_readfirstlane(gv.x);
        SB();
        RCY(d_rdy);
        if (__builtin_amdgcn_readfirstlane(gv.y)) wait_cy(g2);
        unsigned ov;
#pragma unroll 1
        for (unsigned spins = 0;; ++spins) {
          v4u pv[10];
#pragma unroll
          for (int u = 0; u < 10; ++u) pv[u] = ld16(rx, po[u], sbase);
          SB();
          f32x4 d0 = Z4, d1 = Z4;
          unsigned m = 0;
#pragma unroll
          for (int u = 0; u < 10; ++u) {
            d0 = mf32(ua[2 * u], pv[u], d0);
            d1 = mf32(ua[2 * u + 1], pv[u], d1);
            m = max(m, umax4(pv[u]));
            SB();
          }
          ov = min(cvtpk(((d0[0] + dppf<0x102>(d0[1])) + dppf<0x104>(d0[2])) + dppf<0x106>(d0[3]),
                         ((d1[0] + dppf<0x102>(d1[1])) + dppf<0x104>(d1[2])) + dppf<0x106>(d1[3])), 0xFFFFFFFEu);
          asm volatile("" ::: "memory");
          if (!__any(m == 0xFFFFFFFFu) || cx.dead) break;
          if ((spins & 63u) == 63u) {
            if (spins > 20000u) __hip_atomic_store(cx.abortw, 1u, RLX_AGENT);
            if (__builtin_amdgcn_readfirstlane(__hip_atomic_load(cx.abortw, RLX_AGENT))) cx.dead = true;
          }
        }
        if ((lane & 6) == 0) st4(ov, rx, ooff, nbase);
        SB();
        if (w == 0) {
          const unsigned cpub = cy32();
          if (lane == 0) st4(min(ck.time_of(cpub), 0xFFFFFFFEu), rx, xmap(XT2 + cg * 16), nbase);
          ldsst<unsigned>(L0 + L2_MISC, cpub);
          RD(32 + cg, s, 0, g2 - d_rdy); RD(32 + cg, s, 1, cpub - g2);
        }
      }
      barx();
      tref = __builtin_amdgcn_readfirstlane(ldsld<unsigned>(L0 + L2_MISC));
    }
    if (w == 3) {
#pragma unroll
      for (int u = 0; u < 4; ++u) st16(ldsld<v4u>(L0 + L2_UM + u * 1024 + lane * 16), rum, cg * 4096 + u * 1024 + lane * 16, s * (80 * 4096));
      ufv = ld4(cx.rc, ufo, 0);
    }
  }
  if (w == 3) { VMWAIT(0); if (lane == 0) flag_set(cx, C_MF2 + cg * 4, NSEG); }
}

enum { R_K = 0, R_V = 1, R_Q = 2, R_F1 = 3 };
constexpr int RING = 40960, Q_PART = 3 * RING;
constexpr int K_LEAD = 6, U_LEAD = 6, V_LEAD = 9;

__device__ __forceinline__ void load_w192(const u16* W, int ldw, v4u (&wr)[20][3], unsigned L0, int w, int lane) {
  const unsigned trb = (8 * (lane >> 4) + ((lane & 15) >> 2)) * 384 + (3 * w) * 32 + (lane & 3) * 8;
  stage384((const char*)W, ldw * 2, 0, 20, L0, w, lane);
  stage384((const char*)W, ldw * 2, 160, 20, L0 + 61440, w, lane);
#pragma unroll
  for (int c = 0; c < 4; ++c) {
    if (c < 3) VMWAIT(15); else VMWAIT(0);
    barx();
#pragma unroll
    for (int ks = 0; ks < 5; ++ks)
#pragma unroll
      for (int t = 0; t < 3; ++t) { const unsigned ad = L0 + (c & 1) * 61440 + trb + ks * 32 * 384 + t * 32; wr[5 * c + ks][t] = cat(trrd(ad), trrd(ad + 4 * 384)); }
    barx();
    if (c < 2) stage384((const char*)W, ldw * 2, 160 * (c + 2), 20, L0 + (c & 1) * 61440, w, lane);
  }
}
__device__ __forceinline__ void ring_issue(const char* src, int s, unsigned L0, int w) {
  const char* p = src + (size_t)s * RING;
  const unsigned d = L0 + (s % 3) * RING + w * 10240;
  dma16x4(p, d); dma16x4(p + 4096, d + 4096); dma16x2(p + 8192, d + 8192);
}
__device__ __forceinline__ void stagger(int w) {
  for (int i = 0; i < w; ++i) asm volatile("s_nop 7\n\ts_nop 3");
}
template <int OFF> __device__ __forceinline__ void dma16o(const char* gsrc) {
  asm volatile("global_load_lds_dwordx4 %0, off offset:%1 sc1" :: "v"(gsrc), "i"(OFF) : "memory");
}
__device__ __forceinline__ void set_m0(unsigned v) { asm volatile("s_mov_b32 m0, %0\n\ts_nop 0" :: "s"(v) : "memory"); }
__device__ __forceinline__ void gemm192(const v4u (&wr)[20][3], unsigned rb, f32x4 (&acc)[2][3], bool fill, const char* fsrc, unsigned fdst) {
  const f32x4 Z4 = {0.f, 0.f, 0.f, 0.f};
#pragma unroll
  for (int mt = 0; mt < 2; ++mt)
#pragma unroll
    for (int t = 0; t < 3; ++t) acc[mt][t] = Z4;
  stagger(__builtin_amdgcn_readfirstlane(threadIdx.x >> 6));
  v4u hb[4];
  hb[0] = ldsld<v4u>(rb); hb[1] = ldsld<v4u>(rb + 20480); hb[2] = ldsld<v4u>(rb + 1024); hb[3] = ldsld<v4u>(rb + 20480 + 1024);
#pragma unroll
  for (int ks = 0; ks < 20; ++ks)
#pragma unroll
    for (int mt = 0; mt < 2; ++mt) {
      const v4u hv = hb[(2 * ks + mt) & 3];
#pragma unroll
      for (int t = 0; t < 3; ++t) acc[mt][t] = mf32(wr[ks][t], hv, acc[mt][t]);
      SB();
      if (ks + 2 < 20) hb[(2 * ks + mt) & 3] = ldsld<v4u>(rb + mt * 20480 + (ks + 2) * 1024);
      if (mt == 1 && (ks & 1) == 0 && fill) {
        if ((ks & 7) == 0) set_m0(fdst + (ks >> 3) * 4096);
        const char* fp = fsrc + (ks >> 3) * 4096;
        if ((ks & 7) == 0) dma16o<0>(fp); else if ((ks & 7) == 2) dma16o<1024>(fp); else if ((ks & 7) == 4) dma16o<2048>(fp); else dma16o<3072>(fp);
      }
      SB();
    }
}

constexpr int PU_LDS = 3 * RING, PU_MISC = PU_LDS + 32768;
__device__ __forceinline__ void pu_task(const Args& a, const int task, const unsigned L0, Ctx& cx) {
  const int tid = threadIdx.x, lane = tid & 63, w = __builtin_amdgcn_readfirstlane(tid >> 6);
  const rsrc_t rux = mkrs(a.ws + O_UX, (size_t)S * H * DM * 2), rum = mkrs(a.ws + O_UM, (size_t)NSEG * 80 * 4096), rx1 = mkrs(a.ws + O_X1, SZ_ACT);
  const rsrc_t rpx = mkrs(a.ws + O_PX, (size_t)NSEG * 65536), rpm = mkrs(a.ws + O_PM, (size_t)NSEG * 16384);
  const float* mod = (const float*)(a.ws + O_MOD);
  const unsigned P0 = L0 + PU_LDS;
  const f32x4 Z4 = {0.f, 0.f, 0.f, 0.f};
  const int s = task / 10, part = task % 10;
  const unsigned lo = (((lane & 15) >> 3) + part * 8) , li = (lane & 1) * 1024 + (4 * ((lane & 7) >> 1) + (lane >> 4)) * 16;
  const unsigned uxo = lo * 16384 + li + w * 4096, umo = lo * 4096 + li + (w >> 1) * 2048 + (w & 1) * 512;
  const unsigned usb = s * (80 * 16384);
  const int col = part * 64 + w * 16 + 4 * (lane >> 4);
  const f32x4 g = *(const f32x4*)(mod + 2 * DM + col);
  const v2u xr0 = *(const v2u*)(a.x + (size_t)(s * 32 + (lane & 15)) * DM + col), xr1 = *(const v2u*)(a.x + (size_t)(s * 32 + 16 + (lane & 15)) * DM + col);
  v4u u[2][2][4];
#define PU_LDU(buf, k2)                                                                                                     \
  _Pragma("unroll") for (int k = 0; k < 2; ++k) _Pragma("unroll") for (int nt = 0; nt < 4; ++nt)                            \
    u[buf][k][nt] = (k2) < 4 ? ld16(rux, uxo + nt * 32768 + (((k2) * 2 + k) >> 2) * 2048 + (((k2) * 2 + k) & 3) * 256, usb) \
                             : ld16(rum, umo + nt * 8192 + k * 256, s * (80 * 4096));
  v4u p[10][2];
#define PU_LDP(k0, k1)                                                                                                      \
  _Pragma("unroll") for (int k = k0; k < k1; ++k) _Pragma("unroll") for (int mt = 0; mt < 2; ++mt)                          \
    p[k][mt] = k < 8 ? ld16(rpx, (mt * 32 + 8 * w + k) * 1024 + lane * 16, s * 65536) : ld16(rpm, (mt * 8 + 2 * w + k - 8) * 1024 + lane * 16, s * 16384);
  PU_LDP(0, 4)
  flags_wait(cx, lane < 8 ? (C_UF + (8 * w + lane) * 4) * 4 : (C_MF2 + (part * 8 + (lane & 7)) * 4) * 4, s + 1);
  PU_LDU(0, 0) PU_LDU(1, 1) PU_LDP(4, 10)
#undef PU_LDP
  SB();
  f32x4 d[2][4];
#pragma unroll
  for (int mt = 0; mt < 2; ++mt)
#pragma unroll
    for (int nt = 0; nt < 4; ++nt) d[mt][nt] = Z4;
#define PU_MM(buf, k2)                                                                                                      \
  _Pragma("unroll") for (int k = 0; k < 2; ++k) _Pragma("unroll") for (int nt = 0; nt < 4; ++nt) {                          \
    d[0][nt] = mf32(u[buf][k][nt], p[(k2) * 2 + k][0], d[0][nt]); d[1][nt] = mf32(u[buf][k][nt], p[(k2) * 2 + k][1], d[1][nt]); } SB();
  PU_MM(0, 0) PU_LDU(0, 2) SB();
  PU_MM(1, 1) PU_LDU(1, 3) SB();
  PU_MM(0, 2) PU_LDU(0, 4) SB();
  PU_MM(1, 3)
  PU_MM(0, 4)
#undef PU_LDU
#undef PU_MM
#pragma unroll
  for (int mt = 0; mt < 2; ++mt)
#pragma unroll
    for (int nt = 0; nt < 4; ++nt) ldsst<f32x4>(P0 + ((w * 4 + nt) * 2 + mt) * 1024 + lane * 16, d[mt][nt]);
  barx();
#pragma unroll
  for (int mt = 0; mt < 2; ++mt) {
    const unsigned pa = P0 + (w * 2 + mt) * 1024 + lane * 16;
    const f32x4 t = (ldsld<f32x4>(pa) + ldsld<f32x4>(pa + 8192)) + (ldsld<f32x4>(pa + 16384) + ldsld<f32x4>(pa + 24576));
    const v2u xr = mt ? xr1 : xr0;
    st8((v2u){cvtpk(blo(xr.x) + rbf(g[0] * rbf(t[0])), bhi(xr.x) + rbf(g[1] * rbf(t[1]))),
              cvtpk(blo(xr.y) + rbf(g[2] * rbf(t[2])), bhi(xr.y) + rbf(g[3] * rbf(t[3])))}, rx1, ((s * 32 + mt * 16 + (lane & 15)) * DM + col) * 2, 0);
  }
  VMWAIT(0); barx();
  if (tid == 0) ldsst<unsigned>(L0 + PU_MISC, __hip_atomic_fetch_add((gu32*)(a.ws + O_CTL) + C_XC + s * 32, 1u, RLX_AGENT));
  barx();
  if (__builtin_amdgcn_readfirstlane(ldsld<unsigned>(L0 + PU_MISC)) == 9u) {
    ln_segment<true>(a.ws + O_X1 + (size_t)s * 32 * DM * 2, a.ws + O_MODP + 2 * DM * 2, mkrs(a.ws + O_H2X, SZ_ACT), s * RING, w, lane);
    VMWAIT(0); barx();
    if (tid == 0) flag_set(cx, C_HG2 + s * 4, 1u);
  }
}
__device__ __forceinline__ int pu_claim(const Args& a, const unsigned L0, Ctx& cx, int h, int limit, int& seg, int& prog) {
  const int tid = threadIdx.x, lane = tid & 63;
  if (tid < 64) {
    const int sg = min(seg + (lane >> 1), NSEG - 1);
    const unsigned v = ld4(cx.rc, lane < 8 ? (C_PT + sg * 4 + (lane & 1)) * 4 : (C_MF1 + h * 32) * 4, 0);
    const unsigned pr = __builtin_amdgcn_readlane(v, 8) >> 2;
    const unsigned open = (unsigned)__ballot(lane < 8 && !(lane & 1) && v < 10u && seg + (lane >> 1) < NSEG);
    const unsigned rdy = (unsigned)__ballot(lane < 8 && (lane & 1) && v != 0u) >> 1;
    const int k = open ? __builtin_ctz(open) : 8;
    int nseg = seg + (k >> 1), t = nseg >= NSEG ? -2 : (open ? -1 : -3);
    if (open && ((rdy >> k) & 1u) && (int)pr <= limit && lane == 0) {
      const unsigned part = __hip_atomic_fetch_add((gu32*)(a.ws + O_CTL) + C_PT + nseg * 4, 1u, RLX_AGENT);
      if (part < 10u) t = nseg * 10 + (int)part; else t = -3;
    }
    if (lane == 0) ldsst<v4u>(L0 + PU_MISC + 32, (v4u){(unsigned)t, pr, (unsigned)nseg, 0u});
  }
  barx();
  const v4u r = ldsld<v4u>(L0 + PU_MISC + 32);
  prog = __builtin_amdgcn_readfirstlane(r.y);
  seg = __builtin_amdgcn_readfirstlane(r.z);
  const int t = (int)__builtin_amdgcn_readfirstlane(r.x);
  barx();
  return t;
}
__device__ __forceinline__ void gap_work(const Args& a, const unsigned L0, Ctx& cx, int h, int s, int lead, int& seg) {
#pragma unroll 1
  for (int it = 0; it < 60000; ++it) {
    int prog;
    const int t = pu_claim(a, L0, cx, h, s - lead, seg, prog);
    if (t >= 0) { pu_task(a, t, L0, cx); continue; }
    if (t == -3) continue;
    if (prog + lead > s || (t == -2 && s >= NSEG)) break;
    __builtin_amdgcn_s_sleep(10);
    if ((it & 255) == 255 && __builtin_amdgcn_readfirstlane(__hip_atomic_load(cx.abortw, RLX_AGENT))) break;
  }
}
__device__ __forceinline__ bool is_ahead(unsigned mnow, int s, int lead, const unsigned L0) {
  if (threadIdx.x == 0) ldsst<unsigned>(L0 + PU_MISC + 16, mnow);
  barx();
  return (int)(__builtin_amdgcn_readfirstlane(ldsld<unsigned>(L0 + PU_MISC + 16)) >> 2) + lead <= s;
}

template <int ROLE>
__device__ __forceinline__ void stream_kv(const Args& a, const int u, const unsigned L0, Ctx& cx) {
  const int tid = threadIdx.x, lane = tid & 63, w = __builtin_amdgcn_readfirstlane(tid >> 6);
  const int LEAD = ROLE == R_K ? a.lk : a.lv;
  v4u wr[20][3];
  load_w192((ROLE == R_K ? a.Wk : a.Wv) + u * DH, HD, wr, L0, w, lane);
  const char* src = a.ws + O_HX + w * 10240 + lane * 16;
  const rsrc_t rdst = mkrs(a.ws + (ROLE == R_K ? O_KX : O_VX), (size_t)S * HD * 2);
  const unsigned fword = (ROLE == R_K ? C_KF : C_VF) + u * 32;
  const char* mfp = a.ws + O_CTL + (C_MF1 + u * 32) * 4;
  STAMP(cx.slot, 1);
  int known = 0;
  seg_wait<C_HF1>(cx, 0, known, lane); ring_issue(src, 0, L0, w);
  seg_wait<C_HF1>(cx, 1, known, lane); ring_issue(src, 1, L0, w);
  bool drained = true;
  unsigned mprev = 0;
#pragma unroll 1
  for (int s = 0; s < NSEG; ++s) {
    if (s == 0) VMWAIT(10);
    else if (drained || s == NSEG - 1) VMWAIT(0);
    else if (ROLE == R_K) VMWAIT(13);
    else VMWAIT(14);
    PIN(mprev);
    if (tid == 0) ldsst<unsigned>(L0 + PU_MISC + 16 + (s & 1) * 4, mprev);
    barx();
    if (!drained && tid == 0) flag_set(cx, fword, s == NSEG - 1 ? s : s - 1);
    const bool ahead = (int)(__builtin_amdgcn_readfirstlane(ldsld<unsigned>(L0 + PU_MISC + 16 + (s & 1) * 4)) >> 2) + LEAD <= s;
    if (s + 2 < NSEG) seg_wait<C_HF1>(cx, s + 2, known, lane);
    mprev = ld4_async(mfp);
    f32x4 acc[2][3];
    gemm192(wr, L0 + (s % 3) * RING + lane * 16, acc, s + 2 < NSEG, src + (size_t)(s + 2) * RING, L0 + ((s + 2) % 3) * RING + w * 10240);
    const unsigned sb = s * (H * 12288);
    if (ROLE == R_K) {
      const unsigned o = (u * 4 + w) * 3072 + lane * 16;
      st16(cat(pack4(acc[0][0]), pack4(acc[0][1])), rdst, o, sb);
      st16(cat(pack4(acc[1][0]), pack4(acc[1][1])), rdst, o + 1024, sb);
      st16(cat(pack4(acc[0][2]), pack4(acc[1][2])), rdst, o + 2048, sb);
    } else {
      const unsigned o = u * 12288 + lane * 16;
#pragma unroll
      for (int mt = 0; mt < 2; ++mt) {
        st16(cat(pack4(acc[mt][0]), pack4(acc[mt][1])), rdst, o + mt * 6144 + w * 1024, sb);
        st8(pack4(acc[mt][2]), rdst, o + mt * 6144 + (4 + (w >> 1)) * 1024 + (w & 1) * 8, sb);
      }
    }
    drained = s < 2 || s == NSEG - 1 || ahead;
    if (drained) {
      VMWAIT(0); barx();
      if (tid == 0) flag_set(cx, fword, s + 1);
    }
    STAMP(cx.slot, 8 + s);
    if (ahead && s < NSEG - 1) { PIN(mprev); gap_work(a, L0, cx, u, s, LEAD, cx.seg); mprev = 0xFFFFu; }
  }
}

__device__ __forceinline__ void stream_f1(const Args& a, const int u, const unsigned L0, Ctx& cx) {
  const int tid = threadIdx.x, lane = tid & 63, w = __builtin_amdgcn_readfirstlane(tid >> 6);
  v4u wr[20][3];
  load_w192(a.w1 + u * DH, FF, wr, L0, w, lane);
  const char* src = a.ws + O_H2X + w * 10240 + lane * 16;
  const char* hfp = a.ws + O_CTL + (C_HG2 + lane * 4) * 4;
  const rsrc_t rdst = mkrs(a.ws + O_FX, (size_t)S * FF * 2);
  float bias[3][4];
#pragma unroll
  for (int t = 0; t < 3; ++t)
#pragma unroll
    for (int r = 0; r < 4; ++r) bias[t][r] = bf2f(a.b1[u * DH + 16 * (3 * w + t) + 4 * (lane >> 4) + r]);
  STAMP(cx.slot, 1);
  int issued = 0, known = 0;
  bool owe = false;
#pragma unroll 1
  for (int s = 0; s < NSEG; ++s) {
    if (issued == s) { seg_wait<C_HG2>(cx, s, known, lane); ring_issue(src, s, L0, w); issued = s + 1; VMWAIT(0); }
    barx();
    const bool fill = issued < known && issued <= s + 2;
    unsigned fv = ld4_async(hfp);
    f32x4 acc[2][3];
    gemm192(wr, L0 + (s % 3) * RING + lane * 16, acc, fill, src + (size_t)issued * RING, L0 + (issued % 3) * RING + w * 10240);
    if (fill) ++issued;
#pragma unroll
    for (int mt = 0; mt < 2; ++mt)
#pragma unroll
      for (int t = 0; t < 3; ++t)
#pragma unroll
        for (int r = 0; r < 4; r += 2) {
          const unsigned p = cvtpk(acc[mt][t][r], acc[mt][t][r + 1]);
          const unsigned q = cvtpk(blo(p) + bias[t][r], bhi(p) + bias[t][r + 1]);
          const float v0 = __builtin_amdgcn_fmed3f(blo(q), 0.f, 3.4e38f), v1 = __builtin_amdgcn_fmed3f(bhi(q), 0.f, 3.4e38f);
          acc[mt][t][r] = v0 * v0; acc[mt][t][r + 1] = v1 * v1;
        }
    VMWAIT(0);
    PIN(fv);
    if (owe && lane == 0) __hip_atomic_fetch_add((gu32*)(a.ws + O_CTL) + C_FF + u * 4, 1u, RLX_AGENT);
    { const unsigned long long miss = __ballot(fv == 0u); known = miss ? __builtin_ctzll(miss) : 64; }
    if (issued == s + 1 && issued < known) { ring_issue(src, issued, L0, w); ++issued; }
    const unsigned o = u * 12288 + lane * 16, sb = s * (8 * 12288);
#pragma unroll
    for (int mt = 0; mt < 2; ++mt) {
      st16(cat(pack4(acc[mt][0]), pack4(acc[mt][1])), rdst, o + mt * 6144 + w * 1024, sb);
      st8(pack4(acc[mt][2]), rdst, o + mt * 6144 + (4 + (w >> 1)) * 1024 + (w & 1) * 8, sb);
    }
    owe = fill && issued > s + 1;
    if (!owe) {
      VMWAIT(0);
      if (lane == 0) __hip_atomic_fetch_add((gu32*)(a.ws + O_CTL) + C_FF + u * 4, 1u, RLX_AGENT);
    }
    STAMP(cx.slot, 8 + s);
  }
}

__device__ __forceinline__ unsigned q_arrive_async(const Args& a, int s) {
  unsigned v = 1u;
  asm volatile("global_atomic_add %0, %1, %0, off sc0" : "+v"(v) : "v"(a.ws + O_CTL + (C_XC + s * 32 + 16) * 4) : "memory");
  return v;
}
__device__ __forceinline__ void stream_q(const Args& a, const int u, const unsigned L0, Ctx& cx) {
  const int tid = threadIdx.x, lane = tid & 63, w = __builtin_amdgcn_readfirstlane(tid >> 6);
  v4u wr[20][3];
  load_w192(a.Wq + u * DH, HD, wr, L0, w, lane);
  const char* src = a.ws + O_HX + w * 10240 + lane * 16;
  const rsrc_t rpx = mkrs(a.ws + O_PX, (size_t)NSEG * 65536), rpm = mkrs(a.ws + O_PM, (size_t)NSEG * 16384);
  const rsrc_t rk = mkrs(a.ws + O_KX, (size_t)S * HD * 2), rkm = mkrs(a.ws + O_KM, (size_t)NSEG * H * 6144);
  const unsigned kfo = (C_KF + u * 32) * 4, mfo = (C_MF1 + u * 32) * 4, ko = (u * 4 + w) * 3072 + lane * 16, mo = (u * 4 + w) * 1536;
  const unsigned fo = (lane & 1) ? kfo : mfo;
  const int smt = w >> 1, g = lane & 7, c16 = 8 * (w & 1) + (lane >> 3), last = 16 * smt + c16 - 4 * g;
  const unsigned pbuf = L0 + Q_PART;
  const unsigned rxa = pbuf + (smt * 3 + (g >> 2)) * 1024 + ((g & 3) * 16 + c16) * 16;
  const unsigned rma = pbuf + (smt * 3 + 2) * 1024 + ((2 + (g >> 2)) * 16 + c16) * 16 + (g & 3) * 4;
  const unsigned sxo = (smt * 32 + u) * 1024 + ((g & 3) * 16 + c16) * 16 + (g >> 2) * 8;
  const unsigned smo = (smt * 8 + (u >> 2)) * 1024 + ((u & 3) * 16 + c16) * 16 + g * 2;
  const f32x4 Z4 = {0.f, 0.f, 0.f, 0.f};
  STAMP(cx.slot, 1);
  int known = 0;
  seg_wait<C_HF1>(cx, 2, known, lane);
  ring_issue(src, 0, L0, w); ring_issue(src, 1, L0, w);
  v4u q01[2]; v2u q2[2];
  unsigned kready = 0, mready = 0, fv = 0, tk = 0;
  int owe = -1, pend = -1;
#pragma unroll 1
  for (int s = -1; s < NSEG; ++s) {
    v4u k0, k1, k2, m01; v2u m2;
    const bool early = s >= 0 && mready >= 4u * (s + 1);
    if (s >= 0) {
      if (kready < (unsigned)s + 1u) flags_wait(cx, kfo, s + 1);
      k0 = ld16(rk, ko, s * (H * 12288)); k1 = ld16(rk, ko + 1024, s * (H * 12288)); k2 = ld16(rk, ko + 2048, s * (H * 12288));
      if (early) { m01 = ld16(rkm, mo + lane * 16, s * (H * 6144)); m2 = ld8(rkm, mo + 1024 + lane * 8, s * (H * 6144)); }
    }
    v4u n01[2]; v2u n2[2];
    if (s + 1 < NSEG) {
      if (s < 0) VMWAIT(10);
      barx();
      if (s + 3 < NSEG) seg_wait<C_HF1>(cx, s + 3, known, lane);
      f32x4 acc[2][3];
      gemm192(wr, L0 + ((s + 1) % 3) * RING + lane * 16, acc, s + 3 < NSEG, src + (size_t)(s + 3) * RING, L0 + ((s + 3) % 3) * RING + w * 10240);
      fv = ld4_async(a.ws + O_CTL + fo);
#pragma unroll
      for (int mt = 0; mt < 2; ++mt) { n01[mt] = cat(pack4(acc[mt][0]), pack4(acc[mt][1])); n2[mt] = pack4(acc[mt][2]); }
    }
    VMWAIT(0);
    PIN(fv);
    mready = __builtin_amdgcn_readlane(fv, 0); kready = __builtin_amdgcn_readlane(fv, 1);
    PIN(tk);
    if (pend >= 0 && tid == 0 && tk == 31u) flag_set(cx, C_PT + pend * 4 + 1, 1u);
    pend = -1;
    if (s >= 0) {
      if (tid == 0) ldsst<unsigned>(pbuf + 24576, mready);
      if (!early) {
        if (mready < 4u * (s + 1)) flags_wait(cx, mfo, 4 * (s + 1));
        m01 = ld16(rkm, mo + lane * 16, s * (H * 6144)); m2 = ld8(rkm, mo + 1024 + lane * 8, s * (H * 6144));
      }
      const v4u qa0 = {q2[0].x, q2[0].y, 0u, 0u}, qa1 = {q2[1].x, q2[1].y, 0u, 0u}, qb1 = {0u, 0u, q2[1].x, q2[1].y}, mz = {m2.x, m2.y, 0u, 0u};
      f32x4 t0 = mf32(k0, q01[0], Z4), t1 = mf32(k0, q01[1], Z4), t2 = mf32(k1, q01[1], Z4), t3 = mf32(m01, q01[0], Z4), t4 = mf32(m01, q01[1], Z4);
      t0 = mf32(k2, qa0, t0); t1 = mf32(k2, qa1, t1); t2 = mf32(k2, qb1, t2); t3 = mf32(mz, qa0, t3); t4 = mf32(mz, qa1, t4);
      const unsigned pa = pbuf + w * 6144 + lane * 16;
      ldsst<f32x4>(pa, t0); ldsst<f32x4>(pa + 3072, t1); ldsst<f32x4>(pa + 4096, t2); ldsst<f32x4>(pa + 2048, t3); ldsst<f32x4>(pa + 5120, t4);
      barx();
      if (owe >= 0) { if (tid == 0) tk = q_arrive_async(a, owe); pend = owe; owe = -1; }
      const bool behind = __builtin_amdgcn_readfirstlane(ldsld<unsigned>(pbuf + 24576)) >= 4u * (s + 2) && s + 2 < NSEG;
      const f32x4 xs = (ldsld<f32x4>(rxa) + ldsld<f32x4>(rxa + 6144)) + (ldsld<f32x4>(rxa + 12288) + ldsld<f32x4>(rxa + 18432));
      const float ms = (ldsld<float>(rma) + ldsld<float>(rma + 6144)) + (ldsld<float>(rma + 12288) + ldsld<float>(rma + 18432));
      const unsigned a0 = cvtpk(xs[0], xs[1]), a1 = cvtpk(xs[2], xs[3]), a2 = cvtpk(ms, ms);
      const unsigned b0 = cvtpk(blo(a0) * SCALE, bhi(a0) * SCALE), b1 = cvtpk(blo(a1) * SCALE, bhi(a1) * SCALE), b2 = cvtpk(blo(a2) * SCALE, 0.f);
      const float NEG = -3.0e38f;
      const float v0 = last >= 0 ? blo(b0) : NEG, v1 = last >= 1 ? bhi(b0) : NEG, v2 = last >= 2 ? blo(b1) : NEG, v3 = last >= 3 ? bhi(b1) : NEG, v4 = blo(b2);
      float mx = __builtin_amdgcn_fmed3f(__builtin_amdgcn_fmed3f(v0, v1, 3.4e38f), __builtin_amdgcn_fmed3f(v2, v3, 3.4e38f), 3.4e38f);
      mx = __builtin_amdgcn_fmed3f(mx, v4, 3.4e38f);
      mx = __builtin_amdgcn_fmed3f(mx, dppf<0xB1>(mx), 3.4e38f);
      mx = __builtin_amdgcn_fmed3f(mx, dppf<0x4E>(mx), 3.4e38f);
      mx = __builtin_amdgcn_fmed3f(mx, dppf<0x141>(mx), 3.4e38f);
      const float e0 = __builtin_amdgcn_exp2f((v0 - mx) * LOG2E), e1 = __builtin_amdgcn_exp2f((v1 - mx) * LOG2E), e2 = __builtin_amdgcn_exp2f((v2 - mx) * LOG2E),
                  e3 = __builtin_amdgcn_exp2f((v3 - mx) * LOG2E), e4 = __builtin_amdgcn_exp2f((v4 - mx) * LOG2E);
      float sum = ((e0 + e1) + (e2 + e3)) + e4;
      sum += dppf<0xB1>(sum); sum += dppf<0x4E>(sum); sum += dppf<0x141>(sum);
      const float inv = __builtin_amdgcn_rcpf(sum);
      st8((v2u){cvtpk(e0 * inv, e1 * inv), cvtpk(e2 * inv, e3 * inv)}, rpx, sxo, s * 65536);
      const float pm = e4 * inv, po = dppf<0xB1>(pm);
      if (!(g & 1)) st4(cvtpk(pm, po), rpm, smo, s * 16384);
      if (behind) owe = s;
      else {
        VMWAIT(0);
        PIN(tk);
        barx();
        if (tid == 0) {
          if (pend >= 0 && tk == 31u) flag_set(cx, C_PT + pend * 4 + 1, 1u);
          if (__hip_atomic_fetch_add((gu32*)(a.ws + O_CTL) + C_XC + s * 32 + 16, 1u, RLX_AGENT) == 31u) flag_set(cx, C_PT + s * 4 + 1, 1u);
        }
        pend = -1;
      }
      STAMP(cx.slot, 8 + s);
    }
#pragma unroll
    for (int mt = 0; mt < 2; ++mt) { q01[mt] = n01[mt]; q2[mt] = n2[mt]; }
  }
}

__device__ __forceinline__ void unit_u(const Args& a, const int h, const unsigned L0, Ctx& cx) {
  const int tid = threadIdx.x, lane = tid & 63, w = __builtin_amdgcn_readfirstlane(tid >> 6);
  v2u wh[12][10];
  {
    const char* W = (const char*)(a.Wo + (size_t)h * DH * DM);
    const unsigned trb = (4 * (lane >> 4) + ((lane & 15) >> 2)) * 1280 + w * 320 + (lane & 3) * 8;
    auto stage = [&](int c) {
#pragma unroll 1
      for (int i = w; i < 60; i += 4) dma16p(W + c * 61440 + i * 1024 + lane * 16, L0 + (c & 1) * 61440 + i * 1024);
    };
    stage(0); stage(1);
#pragma unroll
    for (int c = 0; c < 4; ++c) {
      if (c < 3) VMWAIT(15); else VMWAIT(0);
      barx();
#pragma unroll
      for (int t = 0; t < 3; ++t)
#pragma unroll
        for (int nt = 0; nt < 10; ++nt) wh[3 * c + t][nt] = trrd(L0 + (c & 1) * 61440 + trb + t * 16 * 1280 + nt * 32);
      barx();
      if (c < 2) stage(c + 2);
    }
  }
  const char* src = a.ws + O_VX + (size_t)h * 12288 + w * 3072 + lane * 16;
  const rsrc_t rdst = mkrs(a.ws + O_UX, (size_t)S * H * DM * 2);
  const unsigned vfo = (C_VF + h * 32) * 4;
  const unsigned so = ((lane & 15) >> 3) * 16384 + (h >> 2) * 2048 + (lane & 1) * 1024 + ((h & 3) * 16 + 4 * ((lane & 7) >> 1) + (lane >> 4)) * 16 + w * (20 * 16384);
  auto issue = [&](int s) {
    dma16x2(src + (size_t)s * (H * 12288), L0 + (s & 3) * 12288 + w * 3072);
    dma16(src + (size_t)s * (H * 12288) + 2048, L0 + (s & 3) * 12288 + w * 3072 + 2048);
  };
  STAMP(cx.slot, 1);
  int issued = 0;
  bool drained = true;
  unsigned mprev = 0;
  const f32x4 Z4 = {0.f, 0.f, 0.f, 0.f};
#pragma unroll 1
  for (int s = 0; s < NSEG; ++s) {
    if (issued == s) { flags_wait(cx, vfo, s + 1); issue(s); issued = s + 1; VMWAIT(0); }
    else if (drained) VMWAIT(0);
    else VMWAIT(5);
    PIN(mprev);
    if (tid == 0) ldsst<unsigned>(L0 + PU_MISC + 16 + (s & 1) * 4, mprev);
    barx();
    if (!drained && tid == 0) flag_set(cx, C_UF + h * 4, s - 1);
    const bool ahead = (int)(__builtin_amdgcn_readfirstlane(ldsld<unsigned>(L0 + PU_MISC + 16 + (s & 1) * 4)) >> 2) + a.lu <= s;
    mprev = ld4_async(a.ws + O_CTL + (C_MF1 + h * 32) * 4);
    const unsigned rb = L0 + (s & 3) * 12288 + lane * 16;
    v4u vf[2][6];
#pragma unroll
    for (int mt = 0; mt < 2; ++mt)
#pragma unroll
      for (int ks = 0; ks < 6; ++ks) vf[mt][ks] = ldsld<v4u>(rb + (mt * 6 + ks) * 1024);
    const unsigned vnow = ld4(cx.rc, vfo, 0);
#pragma unroll
    for (int nt = 0; nt < 10; ++nt) {
      f32x4 d0 = Z4, d1 = Z4;
#pragma unroll
      for (int ks = 0; ks < 6; ++ks) {
        const v4u b = cat(wh[ks < 4 ? 3 * ks : (ks == 4 ? 2 : 8)][nt], wh[ks < 4 ? 3 * ks + 1 : (ks == 4 ? 5 : 11)][nt]);
        d0 = mf32(vf[0][ks], b, d0);
        d1 = mf32(vf[1][ks], b, d1);
      }
      st16(cat(pack4(d0), pack4(d1)), rdst, so + nt * 32768, s * (80 * 16384));
      if (nt == 4 && s + 1 < NSEG && (unsigned)__builtin_amdgcn_readfirstlane(vnow) >= (unsigned)s + 2u) { issue(s + 1); issued = s + 2; }
    }
    drained = s < 2 || s == NSEG - 1 || ahead;
    if (drained) {
      VMWAIT(0); barx();
      if (tid == 0) flag_set(cx, C_UF + h * 4, s + 1);
    }
    STAMP(cx.slot, 8 + s);
    if (ahead && s < NSEG - 1) { PIN(mprev); gap_work(a, L0, cx, h, s, a.lu, cx.seg); mprev = 0xFFFFu; }
  }
}

__device__ __forceinline__ void unit_f2(const Args& a, const int jj, const unsigned L0, Ctx& cx) {
  const int tid = threadIdx.x, lane = tid & 63, w = __builtin_amdgcn_readfirstlane(tid >> 6);
  v2u wh[2][12][5];
  {
    const unsigned trb = w * 30720 + (4 * (lane >> 4) + ((lane & 15) >> 2)) * 160 + (lane & 3) * 8;
#pragma unroll
    for (int c = 0; c < 2; ++c) {
      for (int i = 0; i < 30; ++i) {
        const unsigned idx = i * 64 + lane, row = idx / 10, ch = idx % 10;
        dma16p(a.w2 + (size_t)(384 * w + 192 * c + row) * DM + jj * 80 + ch * 8, L0 + w * 30720 + i * 1024);
      }
      VMWAIT(0); barx();
#pragma unroll
      for (int t = 0; t < 12; ++t)
#pragma unroll
        for (int nt = 0; nt < 5; ++nt) wh[c][t][nt] = trrd(L0 + trb + t * 16 * 160 + nt * 32);
      barx();
    }
  }
  const rsrc_t rf = mkrs(a.ws + O_FX, (size_t)S * FF * 2), rx1 = mkrs(a.ws + O_X1, SZ_ACT);
  const unsigned ffo = (C_FF + (2 * w + (lane & 1)) * 4) * 4, fo = 2 * w * 12288 + lane * 16;
  v2u bb[3]; f32x4 gg[3]; unsigned oo[3];
#pragma unroll
  for (int k = 0; k < 3; ++k) {
    const int i = w + 4 * k < 10 ? w + 4 * k : w, col = jj * 80 + (i % 5) * 16 + 4 * (lane >> 4);
    bb[k] = *(const v2u*)(a.b2 + col);
    gg[k] = *(const f32x4*)((const float*)(a.ws + O_MOD) + 5 * DM + col);
    oo[k] = (((i / 5) * 16 + (lane & 15)) * DM + col) * 2;
  }
  STAMP(cx.slot, 1);
  const f32x4 Z4 = {0.f, 0.f, 0.f, 0.f};
  v4u fa[12], fb[12];
  bool have = false;
#pragma unroll 1
  for (int s = 0; s < NSEG; ++s) {
    const unsigned sb = s * (8 * 12288);
    if (!have) {
      flags_wait(cx, ffo, 4 * (s + 1));
#pragma unroll
      for (int i = 0; i < 12; ++i) fa[i] = ld16(rf, fo + i * 1024, sb);
    }
    const unsigned nf = ld4(cx.rc, ffo, 0);
    v2u xr[3];
    f32x4 acc[2][5];
#pragma unroll
    for (int mt = 0; mt < 2; ++mt)
#pragma unroll
      for (int nt = 0; nt < 5; ++nt) acc[mt][nt] = Z4;
    SB();
#pragma unroll
    for (int ks = 0; ks < 6; ++ks)
#pragma unroll
      for (int nt = 0; nt < 5; ++nt) {
        const v4u wa = cat(wh[0][ks < 4 ? 3 * ks : (ks == 4 ? 2 : 8)][nt], wh[0][ks < 4 ? 3 * ks + 1 : (ks == 4 ? 5 : 11)][nt]);
        acc[0][nt] = mf32(wa, fa[ks], acc[0][nt]);
        acc[1][nt] = mf32(wa, fa[6 + ks], acc[1][nt]);
        SB();
        if (nt == 0) fb[ks] = ld16(rf, fo + 12288 + ks * 1024, sb);
        if (nt == 2) fb[6 + ks] = ld16(rf, fo + 12288 + (6 + ks) * 1024, sb);
        if (nt == 4 && ks < 3) xr[ks] = ld8(rx1, oo[ks], s * (32 * DM * 2));
        SB();
      }
    have = s + 1 < NSEG && !__any(nf < 4u * (s + 2));
    const unsigned nsb = have ? sb + 8 * 12288 : sb;
    SB();
#pragma unroll
    for (int ks = 0; ks < 6; ++ks)
#pragma unroll
      for (int nt = 0; nt < 5; ++nt) {
        const v4u wa = cat(wh[1][ks < 4 ? 3 * ks : (ks == 4 ? 2 : 8)][nt], wh[1][ks < 4 ? 3 * ks + 1 : (ks == 4 ? 5 : 11)][nt]);
        acc[0][nt] = mf32(wa, fb[ks], acc[0][nt]);
        acc[1][nt] = mf32(wa, fb[6 + ks], acc[1][nt]);
        SB();
        if (nt == 1) fa[ks] = ld16(rf, fo + ks * 1024, nsb);
        if (nt == 3) fa[6 + ks] = ld16(rf, fo + (6 + ks) * 1024, nsb);
        SB();
      }
    const unsigned pb = L0 + (s & 1) * 40960;
#pragma unroll
    for (int mt = 0; mt < 2; ++mt)
#pragma unroll
      for (int nt = 0; nt < 5; ++nt) ldsst<f32x4>(pb + (w * 10 + mt * 5 + nt) * 1024 + lane * 16, acc[mt][nt]);
    barx();
#pragma unroll
    for (int k = 0; k < 3; ++k) {
      if (w + 4 * k < 10) {
        const unsigned pa = pb + (w + 4 * k) * 1024 + lane * 16;
        const f32x4 t = (ldsld<f32x4>(pa) + ldsld<f32x4>(pa + 10240)) + (ldsld<f32x4>(pa + 20480) + ldsld<f32x4>(pa + 30720));
        const float o0 = blo(xr[k].x) + rbf(gg[k][0] * rbf(rbf(t[0]) + blo(bb[k].x))), o1 = bhi(xr[k].x) + rbf(gg[k][1] * rbf(rbf(t[1]) + bhi(bb[k].x)));
        const float o2 = blo(xr[k].y) + rbf(gg[k][2] * rbf(rbf(t[2]) + blo(bb[k].y))), o3 = bhi(xr[k].y) + rbf(gg[k][3] * rbf(rbf(t[3]) + bhi(bb[k].y)));
        *(v2u*)((char*)a.out + (size_t)s * (32 * DM * 2) + oo[k]) = (v2u){cvtpk(o0, o1), cvtpk(o2, o3)};
      }
    }
    STAMP(cx.slot, 8 + s);
  }
}

__global__ void __launch_bounds__(256, 1) k_main(Args a) {
  extern __shared__ __attribute__((aligned(16))) char lds_[];
  const unsigned L0 = (unsigned)(size_t)(LAS char*)lds_;
  const int tid = threadIdx.x, lane = tid & 63, w = __builtin_amdgcn_readfirstlane(tid >> 6);
  if (tid == 0) {
    unsigned x = xcc_id() & 7u, slot = 0;
    for (int tries = 0; tries < 8; ++tries) {
      const unsigned rk = __hip_atomic_fetch_add((gu32*)(a.ws + O_CTL) + C_TICK + x * 32, 1u, RLX_AGENT);
      if (rk < 32u) { slot = x * 32u + rk; break; }
      x = (x + 1u) & 7u;
    }
    ldsst<unsigned>(L0, slot);
  }
  barx();
  const unsigned slot = __builtin_amdgcn_readfirstlane(ldsld<unsigned>(L0));
  barx();
  Ctx cx{mkrs(a.ws + O_CTL, 65536), (gu32*)(a.ws + O_CTL) + C_ABORT, false, slot, 0};
  STAMP(slot, 0);
  const unsigned x = slot >> 5, rk = slot & 31u;
  if (x < 4 && rk < 8) { layer1(a, x * 8 + rk, L0, cx); STAMP(slot, 2); barx(); gap_work(a, L0, cx, 0, 1000, 0, cx.seg); STAMP(slot, 3); return; }
  if (x < 4 && rk < 28) { layer2(a, x * 20 + (rk - 8), L0, cx); STAMP(slot, 2); barx(); gap_work(a, L0, cx, 0, 1000, 0, cx.seg); STAMP(slot, 3); return; }
  auto ln1 = [&](int seg) {
    ln_segment<false>((const char*)(a.x + (size_t)seg * 32 * DM), a.ws + O_MODP, mkrs(a.ws + O_HX, SZ_ACT), seg * RING, w, lane);
    VMWAIT(0); barx();
    if (tid == 0) flag_set(cx, C_HF1 + seg * 4, 1u);
  };
  if (x < 4) {
    const int f = x * 4 + (rk - 28);
    ln1(32 + 2 * f); ln1(33 + 2 * f);
    flags_wait(cx, C_UF * 4, 1u);
    if (f < 8) stream_f1(a, f, L0, cx); else unit_f2(a, f - 8, L0, cx);
    STAMP(slot, 2); STAMP(slot, 3);
    return;
  }
  const int h = (x - 4) * 8 + (rk & 7), kind = rk >> 3;
  if (kind == 3) { ln1(h); flags_wait(cx, (C_VF + h * 32) * 4, 1u); }
  if (kind == 0) stream_kv<R_K>(a, h, L0, cx);
  else if (kind == 1) stream_kv<R_V>(a, h, L0, cx);
  else if (kind == 2) unit_u(a, h, L0, cx);
  else { stream_q(a, h, L0, cx); STAMP(slot, 2); STAMP(slot, 3); return; }
  STAMP(slot, 2);
  barx();
  gap_work(a, L0, cx, h, 1000, 0, cx.seg);
  STAMP(slot, 3);
}

extern "C" void kernel_launch(void* const* d_in, const int* in_sizes, int n_in, void* d_out, int out_size, void* d_ws, size_t ws_size, hipStream_t stream) {
  const size_t pad = (size_t)(-(intptr_t)d_ws) & ((2u << 20) - 1u);
  if (ws_size < WS_NEED + pad) return;
  Args a;
  a.x = (const u16*)d_in[0]; a.mem0 = (const u16*)d_in[1]; a.Wq = (const u16*)d_in[2]; a.Wk = (const u16*)d_in[3]; a.Wv = (const u16*)d_in[4]; a.Wo = (const u16*)d_in[5];
  a.w1 = (const u16*)d_in[8]; a.b1 = (const u16*)d_in[9]; a.w2 = (const u16*)d_in[10]; a.b2 = (const u16*)d_in[11]; a.c = (const u16*)d_in[12]; a.wmod = (const u16*)d_in[13];
  a.ws = (char*)d_ws + pad; a.out = (u16*)d_out;
  a.w1q = 90;
  a.w2q = 100;
  a.l2d = 300; a.lk = 8; a.lu = 8; a.lv = 13;
  (void)hipFuncSetAttribute((const void*)k_main, hipFuncAttributeMaxDynamicSharedMemorySize, MAIN_LDS);
  hipLaunchKernelGGL(k_pre, dim3(PRE_GRID), dim3(256), 0, stream, a);
  hipLaunchKernelGGL(k_main, dim3(256), dim3(256), MAIN_LDS, stream, a);
}
```

```cpp
#include <hip/hip_runtime.h>
#include <hip/hip_bf16.h>
#include <stdint.h>
#include <stdlib.h>

typedef unsigned short u16;
typedef __bf16 bf16x2 __attribute__((ext_vector_type(2)));
typedef __bf16 bf16x4 __attribute__((ext_vector_type(4)));
typedef __bf16 bf16x8 __attribute__((ext_vector_type(8)));
typedef __bf16 bf16x16 __attribute__((ext_vector_type(16)));
typedef float f32x2 __attribute__((ext_vector_type(2)));
typedef float f32x4 __attribute__((ext_vector_type(4)));
typedef unsigned v2u __attribute__((ext_vector_type(2)));
typedef unsigned v4u __attribute__((ext_vector_type(4)));
typedef unsigned v8u __attribute__((ext_vector_type(8)));
#define GAS __attribute__((address_space(1)))
#define LAS __attribute__((address_space(3)))
typedef GAS unsigned gu32;
#define RLX_AGENT __ATOMIC_RELAXED, __HIP_MEMORY_SCOPE_AGENT
typedef __amdgpu_buffer_rsrc_t rsrc_t;

constexpr int S = 2048, DM = 640, H = 32, DH = 192, HD = H * DH, NSEG = 64, FF = 1536;
constexpr float SCALE = 0.07216878364870322f, LOG2E = 1.4426950408889634f;

constexpr unsigned XSTEP = 131072;
constexpr unsigned XP = 10240, XT1 = 30720, XT2 = 31232;
__host__ __device__ constexpr unsigned xmap(unsigned o) { return ((o >> 13) << 14) + (o & 8191u); }
constexpr size_t O_XR = 0, O_CTL = O_XR + (size_t)NSEG * XSTEP, O_SEL = O_CTL + 131072, O_MOD = O_SEL + 4096, O_MODP = O_MOD + 16384, O_HX = O_MODP + 8192;
constexpr size_t SZ_ACT = (size_t)S * DM * 2;
constexpr size_t O_H2X = O_HX + SZ_ACT, O_X1 = O_H2X + SZ_ACT, O_KX = O_X1 + SZ_ACT, O_VX = O_KX + (size_t)S * HD * 2, O_UX = O_VX + (size_t)S * HD * 2;
constexpr size_t O_UM = O_UX + (size_t)S * H * DM * 2, O_KM = O_UM + (size_t)NSEG * 80 * 4096, O_PX = O_KM + (size_t)NSEG * H * 6144;
constexpr size_t O_PM = O_PX + (size_t)NSEG * 65536, O_FX = O_PM + (size_t)NSEG * 16384, O_BT = O_FX + (size_t)S * FF * 2, O_DBG = O_BT + (size_t)H * DM * DM * 2;
constexpr size_t WS_NEED = O_DBG + (4u << 20);
constexpr int C_TICK = 0, C_ABORT = 256, C_LNC = 288, C_KF = 512, C_VF = 1536, C_MF1 = 2560, C_UF = 3584, C_PF = 3712, C_MF2 = 3840, C_XC = 4224,
              C_HF2 = 6272, C_FF = 8320, C_PUT = 8416, C_HF1 = 8448, C_HG2 = 8704, C_PT = 8960, C_SCH = 9216, C_WORDS = 9248;

struct Args {
  const u16 *x, *mem0, *Wq, *Wk, *Wv, *Wo, *w1, *b1, *w2, *b2, *c, *wmod;
  char* ws; char* ctl; u16* out;
  int w1q, w2q, l2d, lk, lu, lv;
};

__device__ __forceinline__ float bf2f(u16 v) { return __uint_as_float(((unsigned)v) << 16); }
__device__ __forceinline__ float blo(unsigned v) { return __uint_as_float(v << 16); }
__device__ __forceinline__ float bhi(unsigned v) { return __uint_as_float(v & 0xFFFF0000u); }
__device__ __forceinline__ unsigned cvtpk(float a, float b) { return __builtin_bit_cast(unsigned, __builtin_convertvector((f32x2){a, b}, bf16x2)); }
__device__ __forceinline__ float rbf(float f) { return blo(cvtpk(f, f)); }
template <typename T> __device__ __forceinline__ T ldsld(unsigned a) { return *(LAS T*)(size_t)a; }
template <typename T> __device__ __forceinline__ void ldsst(unsigned a, T v) { *(LAS T*)(size_t)a = v; }
#define SB() __builtin_amdgcn_sched_barrier(0)
__device__ __forceinline__ void barx() { __builtin_amdgcn_s_waitcnt(0xC07F); __builtin_amdgcn_s_barrier(); SB(); }
#define VMWAIT(n) asm volatile("s_waitcnt vmcnt(" #n ")" ::: "memory")
__device__ __forceinline__ rsrc_t mkrs(const void* p, size_t bytes) { return __builtin_amdgcn_make_buffer_rsrc((void*)p, 0, (unsigned)bytes, 0x00020000); }
__device__ __forceinline__ v4u ld16(rsrc_t r, unsigned vo, unsigned so) { return __builtin_amdgcn_raw_buffer_load_b128(r, vo, so, 16); }
__device__ __forceinline__ v2u ld8(rsrc_t r, unsigned vo, unsigned so) { return __builtin_amdgcn_raw_buffer_load_b64(r, vo, so, 16); }
__device__ __forceinline__ unsigned ld4(rsrc_t r, unsigned vo, unsigned so) { return __builtin_amdgcn_raw_buffer_load_b32(r, vo, so, 16); }
__device__ __forceinline__ void st16(v4u v, rsrc_t r, unsigned vo, unsigned so) {
  __builtin_amdgcn_raw_buffer_store_b128(v, r, vo, so, 16);
  asm volatile("s_nop 1" :: "v"(v));
}
__device__ __forceinline__ void st8(v2u v, rsrc_t r, unsigned vo, unsigned so) { __builtin_amdgcn_raw_buffer_store_b64(v, r, vo, so, 16); }
__device__ __forceinline__ void st4(unsigned v, rsrc_t r, unsigned vo, unsigned so) { __builtin_amdgcn_raw_buffer_store_b32(v, r, vo, so, 16); }
__device__ __forceinline__ f32x4 smf(v4u a, v8u b, f32x4 c, int idx) {
  return __builtin_amdgcn_smfmac_f32_16x16x64_bf16(__builtin_bit_cast(bf16x8, a), __builtin_bit_cast(bf16x16, b), c, idx, 0, 0);
}
__device__ __forceinline__ f32x4 mf32(v4u a, v4u b, f32x4 c) {
  return __builtin_amdgcn_mfma_f32_16x16x32_bf16(__builtin_bit_cast(bf16x8, a), __builtin_bit_cast(bf16x8, b), c, 0, 0, 0);
}
__device__ __forceinline__ v2u trrd(unsigned a) { return __builtin_bit_cast(v2u, __builtin_amdgcn_ds_read_tr16_b64_v4bf16((LAS bf16x4*)(size_t)a)); }
__device__ __forceinline__ v2u pack4(f32x4 v) { return (v2u){cvtpk(v[0], v[1]), cvtpk(v[2], v[3])}; }
__device__ __forceinline__ v4u cat(v2u a, v2u b) { return (v4u){a.x, a.y, b.x, b.y}; }
__device__ __forceinline__ v8u cat8(v4u a, v4u b) { return __builtin_shufflevector(a, b, 0, 1, 2, 3, 4, 5, 6, 7); }
__device__ __forceinline__ void dma16(const void* gsrc, unsigned lds_dst) {
  unsigned keep;
  asm volatile("s_mov_b32 %0, m0\n\ts_mov_b32 m0, %2\n\ts_nop 0\n\tglobal_load_lds_dwordx4 %1, off sc1\n\ts_mov_b32 m0, %0"
               : "=&s"(keep) : "v"(gsrc), "s"(lds_dst) : "memory");
}
__device__ __forceinline__ void dma16x4(const void* gsrc, unsigned lds_dst) {
  unsigned keep;
  asm volatile("s_mov_b32 %0, m0\n\ts_mov_b32 m0, %2\n\ts_nop 0\n\tglobal_load_lds_dwordx4 %1, off sc1\n\tglobal_load_lds_dwordx4 %1, off offset:1024 sc1\n\t"
               "global_load_lds_dwordx4 %1, off offset:2048 sc1\n\tglobal_load_lds_dwordx4 %1, off offset:3072 sc1\n\ts_mov_b32 m0, %0"
               : "=&s"(keep) : "v"(gsrc), "s"(lds_dst) : "memory");
}
__device__ __forceinline__ void dma16x2(const void* gsrc, unsigned lds_dst) {
  unsigned keep;
  asm volatile("s_mov_b32 %0, m0\n\ts_mov_b32 m0, %2\n\ts_nop 0\n\tglobal_load_lds_dwordx4 %1, off sc1\n\tglobal_load_lds_dwordx4 %1, off offset:1024 sc1\n\t"
               "s_mov_b32 m0, %0" : "=&s"(keep) : "v"(gsrc), "s"(lds_dst) : "memory");
}
__device__ __forceinline__ void dma16p(const void* gsrc, unsigned lds_dst) {
  unsigned keep;
  asm volatile("s_mov_b32 %0, m0\n\ts_mov_b32 m0, %2\n\ts_nop 0\n\tglobal_load_lds_dwordx4 %1, off nt\n\ts_mov_b32 m0, %0"
               : "=&s"(keep) : "v"(gsrc), "s"(lds_dst) : "memory");
}
__device__ __forceinline__ unsigned xcc_id() { return (unsigned)__builtin_amdgcn_s_getreg(20 | (3 << 11)) & 0xfu; }
__device__ __forceinline__ unsigned umax4(v4u v) { return max(max(v.x, v.y), max(v.z, v.w)); }
__device__ __forceinline__ unsigned rt32() { return (unsigned)__builtin_amdgcn_s_memrealtime(); }
__device__ __forceinline__ unsigned cy32() { return (unsigned)__builtin_amdgcn_s_memtime(); }
__device__ __forceinline__ void wait_cy(unsigned c) { while ((int)(cy32() - c) < 0) {} }
template <int CTRL> __device__ __forceinline__ float dppf(float v) {
  return __builtin_bit_cast(float, __builtin_amdgcn_update_dpp(0, __builtin_bit_cast(int, v), CTRL, 0xf, 0xf, true));
}
__device__ __forceinline__ float row16_sum(float x) { x += dppf<0xB1>(x); x += dppf<0x4E>(x); x += dppf<0x141>(x); x += dppf<0x140>(x); return x; }
__device__ __forceinline__ float xq_max(float x) {
  auto s = __builtin_amdgcn_permlane16_swap(__float_as_uint(x), __float_as_uint(x), false, false);
  x = fmaxf(__uint_as_float(s[0]), __uint_as_float(s[1]));
  auto t = __builtin_amdgcn_permlane32_swap(__float_as_uint(x), __float_as_uint(x), false, false);
  return fmaxf(__uint_as_float(t[0]), __uint_as_float(t[1]));
}
__device__ __forceinline__ float xq_sum(float x) {
  auto s = __builtin_amdgcn_permlane16_swap(__float_as_uint(x), __float_as_uint(x), false, false);
  x = __uint_as_float(s[0]) + __uint_as_float(s[1]);
  auto t = __builtin_amdgcn_permlane32_swap(__float_as_uint(x), __float_as_uint(x), false, false);
  return __uint_as_float(t[0]) + __uint_as_float(t[1]);
}
__host__ __device__ constexpr int smf_col(int qb, int j) {
  return 32 * (j >> 3) + 16 * (qb >> 1) + 8 * (((j & 7) >> 1) & 1) + 4 * (qb & 1) + 2 * ((j & 7) >> 2) + (j & 1);
}

#define STAMP(slot_, i_) do {} while (0)
#define RD(role_, s_, i_, v_) do {} while (0)
#define RCY(x) do {} while (0)
struct Ctx { rsrc_t rc; gu32* abortw; bool dead; unsigned slot; int seg; };
__device__ __forceinline__ bool flags_ready(const Ctx& c, unsigned vo, unsigned need) {
  const unsigned v = ld4(c.rc, vo, 0);
  asm volatile("" ::: "memory");
  return !__any(v < need);
}
__device__ __forceinline__ void flags_wait(Ctx& c, unsigned vo, unsigned need) {
#pragma unroll 1
  for (unsigned spins = 0; !c.dead; ++spins) {
    if (flags_ready(c, vo, need)) return;
    __builtin_amdgcn_s_sleep(4);
    if ((spins & 63u) == 63u) {
      if (spins > 40000u) __hip_atomic_store(c.abortw, 1u, RLX_AGENT);
      if (__builtin_amdgcn_readfirstlane(__hip_atomic_load(c.abortw, RLX_AGENT))) c.dead = true;
    }
  }
}
template <int BASE>
__device__ __forceinline__ void seg_wait(Ctx& c, int s, int& known, int lane) {
#pragma unroll 1
  for (unsigned spins = 0; known <= s && !c.dead; ++spins) {
    const unsigned v = ld4(c.rc, (BASE + lane * 4) * 4, 0);
    asm volatile("" ::: "memory");
    const unsigned long long miss = __ballot(v < (BASE == C_HG2 ? 8u : 1u));
    known = miss ? __builtin_ctzll(miss) : 64;
    if (known > s) return;
    __builtin_amdgcn_s_sleep(4);
    if ((spins & 63u) == 63u) {
      if (spins > 40000u) __hip_atomic_store(c.abortw, 1u, RLX_AGENT);
      if (__builtin_amdgcn_readfirstlane(__hip_atomic_load(c.abortw, RLX_AGENT))) c.dead = true;
    }
  }
}
__device__ __forceinline__ unsigned ld4_async(const void* p) {
  unsigned v;
  asm volatile("global_load_dword %0, %1, off sc1" : "=v"(v) : "v"(p) : "memory");
  return v;
}
#define PIN(x) asm volatile("" : "+v"(x))
__device__ __forceinline__ void flag_set(const Ctx& c, unsigned word, unsigned val) { st4(val, c.rc, word * 4u, 0); }

__global__ void __launch_bounds__(256, 1) k_pre(Args a) {
  __shared__ __attribute__((aligned(16))) char sm[40960];
  const unsigned L0 = (unsigned)(size_t)(LAS char*)sm;
  const int tid = threadIdx.x, lane = tid & 63, w = __builtin_amdgcn_readfirstlane(tid >> 6), b = blockIdx.x;
  if (b >= 60 && b < 77) {
    const int s = (b - 60) * 4 + w;
    const rsrc_t r = mkrs(a.ws + O_XR, (size_t)(NSEG + 1) * XSTEP);
    const unsigned base = min(s, NSEG) * XSTEP + 65536u + (s >= NSEG ? w * 256u : 0u);
    if (lane < 6) st4(0u, r, base + (lane & 1) * 8192u + (lane >> 1) * 2048u, 0);
    VMWAIT(0);
    unsigned best[2] = {0xFFFFFFFFu, 0xFFFFFFFFu};
#pragma unroll 1
    for (int p = 0; p < 3; ++p)
#pragma unroll
      for (int hf = 0; hf < 2; ++hf) {
        const unsigned t0 = cy32();
        asm volatile("s_waitcnt lgkmcnt(0)" ::: "memory");
        const unsigned v = ld4(r, base + hf * 8192u + p * 2048u, 0);
        asm volatile("s_waitcnt vmcnt(0)" :: "v"(v) : "memory");
        best[hf] = min(best[hf], cy32() - t0);
      }
    unsigned nearh = best[1] < best[0] ? 1u : 0u;
    if (xcc_id() >= 4) nearh ^= 1u;
    if (lane == 0) ((unsigned*)(a.ws + O_SEL))[s] = nearh;
  }
  const int h = (b & 7) * 4 + ((b >> 3) & 3), part = b >> 5;
  v4u af[3][4][6];
  auto load_a = [&](int r) {
    const int T = min(w + 4 * r, 9);
#pragma unroll
    for (int tp = 0; tp < 4; ++tp) {
      const int k = 64 * T + smf_col((lane & 15) >> 2, 4 * tp + (lane & 3));
      const u16* ap = a.Wv + (size_t)k * HD + h * DH + 8 * (lane >> 4);
#pragma unroll
      for (int ks = 0; ks < 6; ++ks) af[r][tp][ks] = *(const v4u*)(ap + 32 * ks);
    }
  };
  const rsrc_t rxr = mkrs(a.ws + O_XR, (size_t)NSEG * XSTEP), rbt = mkrs(a.ws + O_BT, (size_t)H * DM * DM * 2);
  {
    v4u wo[8];
#pragma unroll
    for (int k = 0; k < 8; ++k) { const int i = min(tid + 256 * k, 1919); wo[k] = *(const v4u*)(a.Wo + ((size_t)h * DH + i / 10) * DM + part * 80 + (i % 10) * 8); }
    load_a(0); load_a(1);
#pragma unroll
    for (int k = 0; k < 4; ++k) {
      const unsigned i = (unsigned)(b * 256 + tid + k * 65536), s = i >> 12, byte = (i & 4095u) * 16;
      v4u v = {0xFFFFFFFFu, 0xFFFFFFFFu, 0xFFFFFFFFu, 0xFFFFFFFFu};
      const unsigned o = ((byte >> 14) << 13) + (byte & 8191u);
      if (s == 0 && o < 10240) {
        v = *(const v4u*)(a.mem0 + ((o >> 4) & 7) * DM + (o >> 7) * 8);
        for (int q = 0; q < 4; ++q) v[q] = min(v[q], 0xFFFFFFFEu);
      }
      st16(v, rxr, s * XSTEP + byte, 0);
    }
    if (b >= 128 && (b - 128) * 256 + tid < C_WORDS + 2048) ((unsigned*)(a.ws + O_CTL))[(b - 128) * 256 + tid] = 0u;
#pragma unroll
    for (int k = 0; k < 8; ++k) { const int i = min(tid + 256 * k, 1919); ldsst<v4u>(L0 + i * 16, wo[k]); }
  }
  if (b < 60) {
    const int n0 = b * 64 + (tid & 7) * 8, k0 = (tid >> 3) * 20;
    float acc[8] = {0.f, 0.f, 0.f, 0.f, 0.f, 0.f, 0.f, 0.f};
    v4u wv[20];
#pragma unroll
    for (int k = 0; k < 20; ++k) wv[k] = *(const v4u*)(a.wmod + (size_t)(k0 + k) * (6 * DM) + n0);
#pragma unroll
    for (int k = 0; k < 20; ++k) {
      const float cv = bf2f(a.c[k0 + k]);
      const float sc = rbf(cv / (1.f + __expf(-cv)));
#pragma unroll
      for (int j = 0; j < 4; ++j) { acc[2 * j] += sc * blo(wv[k][j]); acc[2 * j + 1] += sc * bhi(wv[k][j]); }
    }
#pragma unroll
    for (int j = 0; j < 8; ++j) ldsst<float>(L0 + 32768 + ((tid >> 3) * 64 + (tid & 7) * 8 + j) * 4, acc[j]);
  }
  __syncthreads();
  if (b < 60 && tid < 64) {
    float s = 0.f;
    for (int k = 0; k < 32; ++k) s += ldsld<float>(L0 + 32768 + (k * 64 + tid) * 4);
    const int n = b * 64 + tid, which = n / DM;
    const float v = rbf(s);
    ((float*)(a.ws + O_MOD))[n] = v;
    if (which != 2 && which != 5) {
      const bool isc = which == 1 || which == 4;
      ((u16*)(a.ws + O_MODP))[((which >= 3 ? 2 : 0) + (isc ? 0 : 1)) * DM + n % DM] = (u16)(cvtpk(isc ? 1.f + v : v, 0.f) & 0xFFFFu);
    }
  }
  v4u bw[6][5];
#pragma unroll
  for (int ks = 0; ks < 6; ++ks)
#pragma unroll
    for (int nt = 0; nt < 5; ++nt) {
      const unsigned ad = L0 + (32 * ks + 8 * (lane >> 4) + ((lane & 15) >> 2)) * 160 + nt * 32 + (lane & 3) * 8;
      bw[ks][nt] = cat(trrd(ad), trrd(ad + 4 * 160));
    }
  load_a(2);
#pragma unroll
  for (int r = 0; r < 3; ++r) {
    const int T = w + 4 * r;
    if (T < 10) {
#pragma unroll
      for (int nt = 0; nt < 5; ++nt) {
        f32x4 acc[4];
#pragma unroll
        for (int tp = 0; tp < 4; ++tp) {
          acc[tp] = (f32x4){0.f, 0.f, 0.f, 0.f};
#pragma unroll
          for (int ks = 0; ks < 6; ++ks) acc[tp] = mf32(af[r][tp][ks], bw[ks][nt], acc[tp]);
        }
        const int cg = part * 10 + 2 * nt + ((lane & 15) >> 3);
        const unsigned dst = ((cg * 4 + (h >> 3)) * 40 + T * 4 + ((h >> 1) & 3)) * 2048 + ((lane >> 4) * 16 + (h & 1) * 8 + (lane & 7)) * 16;
        st16(cat(pack4(acc[0]), pack4(acc[1])), rbt, dst, 0);
        st16(cat(pack4(acc[2]), pack4(acc[3])), rbt, dst + 1024, 0);
      }
    }
  }
}

template <bool COH, int NP>
__device__ __forceinline__ void ln_segment(const char* src, const char* gs, rsrc_t rdst, unsigned dbase, int rbase, int w, int lane) {
  const rsrc_t rs = mkrs(src, 32 * DM * 2);
  const int c16 = lane & 15, row0 = rbase + w * 4 * NP + (lane >> 4);
  v4u v[NP][5], g[5], sh[5];
#pragma unroll
  for (int p = 0; p < NP; ++p)
#pragma unroll
    for (int i = 0; i < 5; ++i) v[p][i] = __builtin_amdgcn_raw_buffer_load_b128(rs, (row0 + 4 * p) * (DM * 2) + (c16 + 16 * i) * 16, 0, COH ? 16 : 0);
#pragma unroll
  for (int i = 0; i < 5; ++i) { g[i] = *(const v4u*)(gs + (c16 + 16 * i) * 16); sh[i] = *(const v4u*)(gs + DM * 2 + (c16 + 16 * i) * 16); }
#pragma unroll
  for (int p = 0; p < NP; ++p) {
    f32x2 f[20];
    f32x2 s2 = {0.f, 0.f};
#pragma unroll
    for (int i = 0; i < 20; ++i) { f[i] = (f32x2){blo(v[p][i >> 2][i & 3]), bhi(v[p][i >> 2][i & 3])}; s2 += f[i]; }
    const float mean = row16_sum(s2.x + s2.y) * (1.f / DM);
    f32x2 q2 = {0.f, 0.f};
#pragma unroll
    for (int i = 0; i < 20; ++i) { const f32x2 d = f[i] - mean; q2 += d * d; }
    const float m = rbf(mean), var = rbf(row16_sum(q2.x + q2.y) * (1.f / DM));
    const float inv = 1.f / rbf(sqrtf(rbf(var + 1e-5f)));
    const int row = row0 + 4 * p;
#pragma unroll
    for (int i = 0; i < 5; ++i) {
      v4u o;
#pragma unroll
      for (int j = 0; j < 4; ++j) {
        const f32x2 dd = f[4 * i + j] - m;
        const unsigned d = cvtpk(dd.x, dd.y);
        const f32x2 nn = (f32x2){blo(d), bhi(d)} * inv;
        const unsigned n = cvtpk(nn.x, nn.y);
        const f32x2 tt = (f32x2){blo(n), bhi(n)} * (f32x2){blo(g[i][j]), bhi(g[i][j])};
        const unsigned t = cvtpk(tt.x, tt.y);
        const f32x2 oo = (f32x2){blo(t), bhi(t)} + (f32x2){blo(sh[i][j]), bhi(sh[i][j])};
        o[j] = cvtpk(oo.x, oo.y);
      }
      const int ch = c16 + 16 * i;
      st16(o, rdst, ((row >> 4) * 20 + (ch >> 2)) * 1024 + ((ch & 3) * 16 + (row & 15)) * 16, dbase);
    }
  }
}

struct Sched {
  unsigned g1, g2, e1, e2; int r1, r2, w1, w2, c1, c2, up; bool v1, v2;
  __device__ __forceinline__ void init(int W1, int W2) { g1 = g2 = e1 = e2 = 0; r1 = r2 = -1; w1 = W1; w2 = W2; c1 = c2 = 0; up = 24 << 6; v1 = v2 = false; }
  __device__ __forceinline__ int upd(int r, int x, int c) const {
    x = min(max(x, 4), 1200);
    const int f = x > r ? min(x, r + up) : x + ((r - x) >> 2);
    return c == 0 ? r : (r < 0 ? x + 100 : (c == 2 ? f : r));
  }
  __device__ __forceinline__ void got_b(unsigned b) {
    const bool ok = v2 && (int)(g2 - b) >= w2 - 12;
    e2 = ok ? g2 : b + w2 + 100; c2 = ok ? 2 : 1;
    r1 = upd(r1, (int)(b - e1), c1);
  }
  __device__ __forceinline__ void got_a(unsigned a) {
    const bool ok = v1 && (int)(g1 - a) >= w1 - 12;
    e1 = ok ? g1 : a + w1 + 100; c1 = ok ? 2 : 1;
    r2 = upd(r2, (int)(a - e2), c2);
    v2 = r1 >= 0; g2 = e1 + r1 + w2;
    v1 = v2 && r2 >= 0; g1 = g2 + r2 + w1;
    up = max(up >> 1, 24);
  }
};
struct Clock {
  unsigned rt0, cy0, rte, cye; float upc, cpu;
  static __device__ __forceinline__ void edge(unsigned& r, unsigned& c) {
    const unsigned r0 = rt32();
#pragma unroll 1
    for (int k = 0; k < 64; ++k) { r = rt32(); c = cy32(); if (r != r0) break; }
  }
  __device__ __forceinline__ void init() { edge(rt0, cy0); rte = rt0; cye = cy0; cpu = 5.9f; upc = 1.f / 5.9f; }
  __device__ __forceinline__ void sync() {
    const unsigned pr = rte, pc = cye;
    edge(rte, cye);
    const float dr = (float)(rte - rt0) * 4.f, dc = (float)(cye - cy0);
    rt0 = pr; cy0 = pc;
    cpu = __builtin_bit_cast(float, __builtin_amdgcn_readfirstlane(__builtin_bit_cast(int, dc * __builtin_amdgcn_rcpf(dr))));
    upc = __builtin_bit_cast(float, __builtin_amdgcn_readfirstlane(__builtin_bit_cast(int, dr * __builtin_amdgcn_rcpf(dc))));
  }
  __device__ __forceinline__ unsigned time_of(unsigned c) const { return rte * 4u + (unsigned)__builtin_amdgcn_readfirstlane((int)((float)(int)(c - cye) * upc)); }
  __device__ __forceinline__ unsigned cycle_of(unsigned t) const { return cye + (unsigned)__builtin_amdgcn_readfirstlane((int)((float)(int)(t - rte * 4u) * cpu)); }
};
#define DPPM " row_mask:0xf bank_mask:0xf\n\t"
__device__ __forceinline__ int wave_max_i32(int v) {
  int x, y;
  asm("s_nop 1\n\t"
      "v_max_i32_dpp %0, %2, %2 quad_perm:[1,0,3,2]" DPPM "s_nop 1\n\t"
      "v_max_i32_dpp %0, %0, %0 quad_perm:[2,3,0,1]" DPPM "s_nop 1\n\t"
      "v_max_i32_dpp %0, %0, %0 row_half_mirror" DPPM "s_nop 1\n\t"
      "v_max_i32_dpp %1, %0, %0 row_mirror" DPPM
      "v_max_i32_dpp %0, %0, %0 row_mirror" DPPM "s_nop 1\n\t"
      "v_permlane16_swap_b32 %0, %1\n\ts_nop 0\n\t"
      "v_max_i32 %0, %0, %1\n\t"
      "v_mov_b32 %1, %0\n\ts_nop 1\n\t"
      "v_permlane32_swap_b32 %0, %1\n\ts_nop 0\n\t"
      "v_max_i32 %0, %0, %1\n\ts_nop 0" : "=&v"(x), "=&v"(y) : "v"(v));
  return __builtin_amdgcn_readfirstlane(x);
}
__device__ __forceinline__ float red32_max(float a, float b) {
  float x, y;
  asm("v_max_f32 %0, %2, %3\n\ts_nop 1\n\t"
      "v_max_f32_dpp %0, %0, %0 quad_perm:[1,0,3,2]" DPPM "s_nop 1\n\t"
      "v_max_f32_dpp %0, %0, %0 quad_perm:[2,3,0,1]" DPPM "s_nop 1\n\t"
      "v_max_f32_dpp %0, %0, %0 row_half_mirror" DPPM "s_nop 1\n\t"
      "v_max_f32_dpp %1, %0, %0 row_mirror" DPPM
      "v_max_f32_dpp %0, %0, %0 row_mirror" DPPM "s_nop 1\n\t"
      "v_permlane16_swap_b32 %0, %1\n\ts_nop 0\n\t"
      "v_max_f32 %0, %0, %1" : "=&v"(x), "=&v"(y) : "v"(a), "v"(b));
  return x;
}
__device__ __forceinline__ float red32_sum(float a, float b) {
  float x, y;
  asm("v_add_f32 %0, %2, %3\n\ts_nop 1\n\t"
      "v_add_f32_dpp %0, %0, %0 quad_perm:[1,0,3,2]" DPPM "s_nop 1\n\t"
      "v_add_f32_dpp %0, %0, %0 quad_perm:[2,3,0,1]" DPPM "s_nop 1\n\t"
      "v_add_f32_dpp %0, %0, %0 row_half_mirror" DPPM "s_nop 1\n\t"
      "v_add_f32_dpp %1, %0, %0 row_mirror" DPPM
      "v_add_f32_dpp %0, %0, %0 row_mirror" DPPM "s_nop 1\n\t"
      "v_permlane16_swap_b32 %0, %1\n\ts_nop 0\n\t"
      "v_add_f32 %0, %0, %1" : "=&v"(x), "=&v"(y) : "v"(a), "v"(b));
  return x;
}
__device__ __forceinline__ void dual_max(int d0, int d1, int& amax, int& bmax) {
  int x, y;
  asm("v_mov_b32 %1, %3\n\tv_mov_b32 %0, %2\n\ts_nop 1\n\t"
      "v_permlane32_swap_b32 %0, %1\n\ts_nop 0\n\t"
      "v_max_i32 %0, %2, %1\n\t"
      "v_max_i32 %0, %0, %3\n\t"
      "v_cndmask_b32 %0, %0, %3, %4\n\ts_nop 1\n\t"
      "v_max_i32_dpp %0, %0, %0 quad_perm:[1,0,3,2]" DPPM "s_nop 1\n\t"
      "v_max_i32_dpp %0, %0, %0 quad_perm:[2,3,0,1]" DPPM "s_nop 1\n\t"
      "v_max_i32_dpp %0, %0, %0 row_half_mirror" DPPM "s_nop 1\n\t"
      "v_max_i32_dpp %1, %0, %0 row_mirror" DPPM
      "v_max_i32_dpp %0, %0, %0 row_mirror" DPPM "s_nop 1\n\t"
      "v_permlane16_swap_b32 %0, %1\n\ts_nop 0\n\t"
      "v_max_i32 %0, %0, %1\n\ts_nop 0" : "=&v"(x), "=&v"(y) : "v"(d0), "v"(d1), "s"(0xFFFFFFFF00000000ull));
  amax = __builtin_amdgcn_readlane(x, 0); bmax = __builtin_amdgcn_readlane(x, 32);
}
struct Stamps { unsigned t0, t1; };
__device__ __forceinline__ Stamps stamps_load(rsrc_t rx, unsigned o0, unsigned o1, unsigned sbase) { return Stamps{ld4(rx, o0, sbase), ld4(rx, o1, sbase)}; }
__device__ __forceinline__ unsigned stamps_latest(Stamps s, rsrc_t rx, unsigned o0, unsigned o1, unsigned sbase, unsigned ref, bool dead) {
#pragma unroll 1
  for (int k = 0; k < 4000; ++k) {
    if (!__any(s.t0 == 0xFFFFFFFFu || s.t1 == 0xFFFFFFFFu) || dead) break;
    __builtin_amdgcn_s_sleep(1);
    asm volatile("" ::: "memory");
    s = stamps_load(rx, o0, o1, sbase);
  }
  return ref + (unsigned)wave_max_i32(max((int)(s.t0 - ref), (int)(s.t1 - ref)));
}
template <int NB>
__device__ __forceinline__ void poll(rsrc_t r, const unsigned (&voff)[NB], unsigned soff, v4u (&v)[NB], Ctx& c) {
#pragma unroll 1
  for (unsigned spins = 0;; ++spins) {
#pragma unroll
    for (int k = 0; k < NB; ++k) v[k] = ld16(r, voff[k], soff);
    unsigned m = 0;
#pragma unroll
    for (int k = 0; k < NB; ++k) m = max(m, umax4(v[k]));
    asm volatile("" ::: "memory");
    if (!__any(m == 0xFFFFFFFFu) || c.dead) return;
    if ((spins & 255u) == 255u) {
      if (spins > 60000u) __hip_atomic_store(c.abortw, 1u, RLX_AGENT);
      if (__builtin_amdgcn_readfirstlane(__hip_atomic_load(c.abortw, RLX_AGENT))) c.dead = true;
    }
  }
}
__device__ __forceinline__ v2u fold_pack(f32x4 a, f32x4 b) {
  float f[4];
#pragma unroll
  for (int r = 0; r < 4; ++r) {
    auto sw = __builtin_amdgcn_permlane32_swap(__float_as_uint(a[r]), __float_as_uint(b[r]), false, false);
    f[r] = __uint_as_float(sw[0]) + __uint_as_float(sw[1]);
  }
  return (v2u){cvtpk(f[0], f[1]), cvtpk(f[2], f[3])};
}

__device__ __forceinline__ void stage384(const char* W, size_t ldb, int row0, int n3, unsigned dst, int w, int lane) {
#pragma unroll 1
  for (int i = w; i < 3 * n3; i += 4) {
    const unsigned byte = (i % 3) * 1024 + lane * 16, row = byte / 384, col = byte % 384;
    dma16p(W + (size_t)(row0 + 8 * (i / 3) + row) * ldb + col, dst + i * 1024);
  }
}

__device__ __forceinline__ unsigned sel_smooth(unsigned m) {
  unsigned r = 0;
#pragma unroll
  for (int g = 0; g < 2; ++g) {
    const unsigned v = (m >> (16 * g)) & 0xFFFFu;
    const int c = __builtin_popcount(v);
    r |= (c >= 11 ? 0xFFFFu : c <= 5 ? 0u : v) << (16 * g);
  }
  return r;
}

constexpr int L1_IMG = 0, L1_PART = 10240, L1_W = L1_PART + 6144, MAIN_LDS = L1_W + 4 * 18 * 2048;
static_assert(MAIN_LDS == 163840, "lds");

template <int W> __device__ constexpr int l1_slot(int T) { return T >= 8 ? T : ((T - W) & 3) == 0 ? (T >> 2) : (T < 4 ? 1 : 4) + ((T - W) & 3); }
template <int W>
__device__ __forceinline__ void l1_weights(const Args& a, const int h, const unsigned L0, const int lane, v8u (&wq)[3][10], v8u (&wk)[3][4]) {
  const unsigned wl = L0 + L1_W + W * 18 * 2048 + lane * 16;
  const unsigned trb = (16 * (lane >> 5) + 4 * ((lane >> 4) & 1) + ((lane & 15) >> 2)) * 384 + (3 * W) * 32 + (lane & 3) * 8;
  auto frag = [&](unsigned base, int t) {
    const unsigned ad = base + trb + t * 32;
    const v2u r0 = trrd(ad), r1 = trrd(ad + 8 * 384), r2 = trrd(ad + 32 * 384), r3 = trrd(ad + 40 * 384);
    return (v8u){r0.x, r1.x, r0.y, r1.y, r2.x, r3.x, r2.y, r3.y};
  };
  auto stage = [&](int r) { stage384((const char*)((r < 5 ? a.Wq : a.Wk) + h * DH), HD * 2, 128 * (r % 5), 16, L0 + (r & 1) * 49152, W, lane); };
  v8u tmp[6][3];
  stage(0); stage(1);
#pragma unroll
  for (int r = 0; r < 10; ++r) {
    if (r < 9) VMWAIT(12); else VMWAIT(0);
    barx();
#pragma unroll
    for (int k = 0; k < 2; ++k)
#pragma unroll
      for (int t = 0; t < 3; ++t) {
        const int T = 2 * (r % 5) + k, sl = l1_slot<W>(T);
        const v8u f = frag(L0 + (r & 1) * 49152 + k * 24576, t);
        if (r < 5) wq[t][sl] = f; else if (T < 4) wk[t][sl == 0 ? 0 : (sl - 1) & 3] = f; else tmp[T < 4 ? 0 : T - 4][t] = f;
      }
    barx();
    if (r < 8) stage(r + 2);
  }
#pragma unroll
  for (int T = 4; T < 10; ++T)
#pragma unroll
    for (int t = 0; t < 3; ++t) {
      const int sl = l1_slot<W>(T), n = sl == 1 ? t : 3 + t * 5 + (sl - 5);
      const v8u f = tmp[T - 4][t];
      ldsst<v4u>(wl + n * 2048, __builtin_shufflevector(f, f, 0, 1, 2, 3));
      ldsst<v4u>(wl + n * 2048 + 1024, __builtin_shufflevector(f, f, 4, 5, 6, 7));
    }
}

__device__ __forceinline__ void layer1(const Args& a, const int h, const unsigned L0, Ctx& cx) {
  const int tid = threadIdx.x, lane = tid & 63, w = __builtin_amdgcn_readfirstlane(tid >> 6);
  const rsrc_t rx = mkrs(a.ws + O_XR, (size_t)NSEG * XSTEP), rk = mkrs(a.ws + O_KX, (size_t)S * HD * 2), rkm = mkrs(a.ws + O_KM, (size_t)NSEG * H * 6144);
  const unsigned wl = L0 + L1_W + w * 18 * 2048 + lane * 16;
  v8u wq[3][10], wk[3][4];
  if (w == 0) l1_weights<0>(a, h, L0, lane, wq, wk); else if (w == 1) l1_weights<1>(a, h, L0, lane, wq, wk);
  else if (w == 2) l1_weights<2>(a, h, L0, lane, wq, wk); else l1_weights<3>(a, h, L0, lane, wq, wk);
  const unsigned long long selmask = __ballot(((const unsigned*)(a.ws + O_SEL))[lane] != 0u);
  const unsigned sel_lo = sel_smooth(__builtin_amdgcn_readfirstlane((unsigned)selmask)), sel_hi = sel_smooth(__builtin_amdgcn_readfirstlane((unsigned)(selmask >> 32)));
  const int sidx = (lane & 8) ? 0xEEEE : 0x4444;
  unsigned moff[3];
#pragma unroll
  for (int k = 0; k < 3; ++k) { int b = w + 4 * k; if (b >= 10) b = w; moff[k] = xmap(b * 1024 + lane * 16); }
  const unsigned img = L0 + L1_IMG + lane * 16, mst2 = img + (w < 2 ? w + 8 : w) * 1024;
  unsigned aad[3];
#pragma unroll
  for (int i = 0; i < 3; ++i) aad[i] = img + ((w + 1 + i) & 3) * 1024;
  const int qi = 2 * w + (lane >> 5), j = lane & 31;
  const unsigned poff = xmap(XP + (w >> 1) * 10240 + (w & 1) * 128 + (lane >> 5) * 16 +
                             ((j & 1) ? (8 + (h >> 4)) * 1024 + ((h >> 2) & 3) * 256 + (h & 3) * 32 + (j - 1) * 2
                                      : (h >> 2) * 1024 + (h & 3) * 256 + ((j & 15) >> 2) * 32 + ((j >> 4) * 4 + (j & 3)) * 2));
  const bool pstore = !(j & 1) || j < 8;
  const unsigned t1o = xmap(XT1 + (lane & 31) * 16), t2o0 = xmap(XT2 + lane * 16), t2o1 = xmap(XT2 + 1024 + (lane & 15) * 16);
  const int dq = (lane & 15) - 4 * (lane >> 4);
  const v4u ident = {dq == 0 ? 0x3F80u : dq == 1 ? 0x3F800000u : 0u, dq == 2 ? 0x3F80u : dq == 3 ? 0x3F800000u : 0u, 0u, 0u};
  const unsigned kxo = (h * 4 + w) * 3072 + lane * 16, kfo = (C_KF + h * 32) * 4;
  STAMP(cx.slot, 1);
  v4u kx[3];
  flags_wait(cx, kfo, 1u);
#pragma unroll
  for (int t = 0; t < 3; ++t) kx[t] = ld16(rk, kxo + t * 1024, 0);
  unsigned kready = __builtin_amdgcn_readfirstlane(ld4(cx.rc, kfo, 0));
  Sched sc; sc.init(a.w1q, a.w2q);
  Clock ck; ck.init();
  unsigned gate = cy32(), pbase = 0;
  const f32x4 Z4 = {0.f, 0.f, 0.f, 0.f};
#pragma unroll 1
  for (int s = 0; s < NSEG; ++s) {
    const unsigned sbase = s * XSTEP + (((s < 32 ? sel_lo : sel_hi) >> (s & 31)) & 1u) * 8192u;
    f32x4 aq[3], ak[3];
#pragma unroll
    for (int t = 0; t < 3; ++t) { aq[t] = Z4; ak[t] = Z4; asm volatile("" : "+v"(aq[t]), "+v"(ak[t])); }
    v4u llo[3], lhi[3];
#pragma unroll
    for (int i = 0; i < 3; ++i) { llo[i] = ldsld<v4u>(wl + i * 2048); lhi[i] = ldsld<v4u>(wl + i * 2048 + 1024); asm volatile("" : "+v"(llo[i]), "+v"(lhi[i])); }
    const unsigned t2x = lane >= 32 ? t1o + pbase : t2o1 + sbase;
    SB();
    RCY(d_rdy);
    wait_cy(gate);
    v4u mv[3];
    poll<3>(rx, moff, sbase, mv, cx);
    RCY(d_det);
    ldsst<v4u>(img + w * 1024, mv[0]);
    ldsst<v4u>(img + (w + 4) * 1024, mv[1]);
    ldsst<v4u>(mst2, mv[2]);
    SB();
#pragma unroll
    for (int t = 0; t < 3; ++t) { aq[t] = smf(mv[0], wq[t][0], aq[t], sidx); ak[t] = smf(mv[0], wk[t][0], ak[t], sidx); }
    SB();
    barx();
    v4u am[8];
#pragma unroll
    for (int i = 0; i < 8; ++i) am[i] = ldsld<v4u>(i < 6 ? aad[i % 3] + (i / 3) * 4096 : img + (i + 2) * 1024);
    SB();
#pragma unroll
    for (int t = 0; t < 3; ++t) {
      aq[t] = smf(mv[1], wq[t][1], aq[t], sidx);
      ak[t] = smf(mv[1], cat8(llo[t], lhi[t]), ak[t], sidx);
      SB();
    }
    if (tid == 192) flag_set(cx, C_MF1 + h * 32, 4u * s);
    SB();
    f32x4 s0 = Z4, s1 = Z4, sm = Z4, xt;
    v4u X01;
    v2u xa;
    unsigned sw[8];
    float ff[4];
#pragma unroll
    for (int t = 0; t < 3; ++t) {
#pragma unroll
      for (int i = 0; i < 8; ++i) {
        const int n = 3 + t * 5 + (i - 3);
        aq[t] = smf(am[i], wq[t][2 + i], aq[t], sidx);
        SB();
        if (t == 0 && i < 3) llo[i] = ldsld<v4u>(wl + (3 + i) * 2048);
        if (i >= 4 && n + 2 < 18) lhi[(n - 1) % 3] = ldsld<v4u>(wl + (n + 2) * 2048 + 1024);
        if (i == 0 && t > 0 && t * 5 + 5 < 18) lhi[(t * 5 + 2) % 3] = ldsld<v4u>(wl + (t * 5 + 5) * 2048 + 1024);
        if (t > 0) {
          if (i == 1) { for (int r = 0; r < 2; ++r) { auto x = __builtin_amdgcn_permlane32_swap(__float_as_uint(aq[t - 1][r]), __float_as_uint(ak[t - 1][r]), false, false); sw[2 * r] = x[0]; sw[2 * r + 1] = x[1]; }
                        ff[0] = __uint_as_float(sw[0]) + __uint_as_float(sw[1]); }
          if (i == 2) { ff[2] = __uint_as_float(sw[4]) + __uint_as_float(sw[5]); ff[3] = __uint_as_float(sw[6]) + __uint_as_float(sw[7]); }
          if (i == 3) xt = mf32((v4u){xa.x, xa.y, 0u, 0u}, ident, Z4);
          if (i == 5) X01[2 * (t - 1) + 1] = cvtpk(xt[2], xt[3]);
          if (t == 2 && i == 6) s0 = mf32(kx[0], X01, s0);
          if (t == 2 && i == 7) sm = mf32(X01, X01, sm);
        }
        SB();
        ak[t] = smf(am[i], i < 3 ? wk[t][i < 3 ? 1 + i : 0] : cat8(llo[(n + 18) % 3], lhi[(n + 18) % 3]), ak[t], sidx);
        SB();
        if (t == 0 && i < 3) lhi[i] = ldsld<v4u>(wl + (3 + i) * 2048 + 1024);
        if (i >= 3 && n + 3 < 18) llo[n % 3] = ldsld<v4u>(wl + (n + 3) * 2048);
        if (t > 0) {
          if (i == 1) { for (int r = 2; r < 4; ++r) { auto x = __builtin_amdgcn_permlane32_swap(__float_as_uint(aq[t - 1][r]), __float_as_uint(ak[t - 1][r]), false, false); sw[2 * r] = x[0]; sw[2 * r + 1] = x[1]; }
                        ff[1] = __uint_as_float(sw[2]) + __uint_as_float(sw[3]); }
          if (i == 2) { xa.x = cvtpk(ff[0], ff[1]); xa.y = cvtpk(ff[2], ff[3]); }
          if (i == 4) X01[2 * (t - 1)] = cvtpk(xt[0], xt[1]);
          if (t == 2 && i == 6) s1 = mf32(kx[1], X01, s1);
        }
        SB();
      }
    }
    xa = fold_pack(aq[2], ak[2]);
    xt = mf32((v4u){xa.x, xa.y, 0u, 0u}, ident, Z4);
    const v2u X2 = pack4(xt);
    unsigned cy_pub = 0, t_pub = 0;
    Stamps st2 = {0u, 0u};
    unsigned t1v = 0;
    if (s < NSEG - 1) {
      const v4u xz = {X2.x, X2.y, 0u, 0u};
      s0 = mf32(kx[2], xz, s0);
      s1 = mf32(kx[2], (v4u){0u, 0u, X2.x, X2.y}, s1);
      sm = mf32(xz, xz, sm);
      if ((lane & 8) == 0) {
        const unsigned pa = L0 + L1_PART + w * 1536 + (lane & 7) * 64 + (lane >> 4) * 16;
        ldsst<f32x4>(pa, s0); ldsst<f32x4>(pa + 512, s1); ldsst<f32x4>(pa + 1024, sm);
      }
      SB();
      st2.t0 = ld4(rx, t2o0, sbase);
      t1v = ld4(rx, t2x, 0);
      SB();
      barx();
      const unsigned ra = L0 + L1_PART + (j >> 4) * 512 + qi * 64 + (j & 15) * 4;
      const unsigned rb = L0 + L1_PART + 1024 + qi * 64 + (8 + (j & 7)) * 4;
      const float x0 = ldsld<float>(ra), x1 = ldsld<float>(ra + 1536), x2 = ldsld<float>(ra + 3072), x3 = ldsld<float>(ra + 4608);
      const float y0 = ldsld<float>(rb), y1 = ldsld<float>(rb + 1536), y2 = ldsld<float>(rb + 3072), y3 = ldsld<float>(rb + 4608);
      SB();
      const unsigned r1 = cvtpk((x0 + x1) + (x2 + x3), (y0 + y1) + (y2 + y3));
      const unsigned r2 = cvtpk(blo(r1) * SCALE, bhi(r1) * SCALE);
      const float sx = blo(r2), sy = bhi(r2);
      const float mx = red32_max(sx, sy);
      const float ex = __builtin_amdgcn_exp2f((sx - mx) * LOG2E), ey = __builtin_amdgcn_exp2f((sy - mx) * LOG2E);
      const float inv = __builtin_amdgcn_rcpf(red32_sum(ex, j < 8 ? ey : 0.f));
      const float own = ((j & 1) ? ey : ex) * inv, snd = ((j & 1) ? ex : ey) * inv;
      const float oth = dppf<0xB1>(snd);
      const unsigned pv = min((j & 1) ? cvtpk(oth, own) : cvtpk(own, oth), 0xFFFFFFFEu);
      if (pstore) st4(pv, rx, poff, sbase);
      SB();
      cy_pub = cy32();
      t_pub = ck.time_of(cy_pub);
      if (tid == 0) st4(min(t_pub, 0xFFFFFFFEu), rx, xmap(XT1 + h * 16), sbase);
    }
    const unsigned kfv = ld4(cx.rc, kfo, 0);
    const bool kpre = s < NSEG - 2 && kready >= (unsigned)s + 2u;
    if (kpre) {
#pragma unroll
      for (int t = 0; t < 3; ++t) kx[t] = ld16(rk, kxo + t * 1024, (s + 1) * (H * 12288));
    }
    st16(X01, rkm, (h * 4 + w) * 1536 + lane * 16, s * (H * 6144));
    st8(X2, rkm, (h * 4 + w) * 1536 + 1024 + lane * 8, s * (H * 6144));
    SB();
    ck.sync();
    if (s < NSEG - 1) {
      if (s > 0) {
        if (__any(st2.t0 == 0xFFFFFFFFu || t1v == 0xFFFFFFFFu)) {
          sc.got_b(stamps_latest(stamps_load(rx, t1o, t1o, pbase), rx, t1o, t1o, pbase, t_pub, cx.dead));
          sc.got_a(stamps_latest(stamps_load(rx, t2o0, t2o1, sbase), rx, t2o0, t2o1, sbase, t_pub, cx.dead));
        } else {
          int am_, bm_;
          dual_max((int)(st2.t0 - t_pub), (int)(t1v - t_pub), am_, bm_);
          sc.got_b(t_pub + (unsigned)bm_);
          sc.got_a(t_pub + (unsigned)am_);
        }
      }
      gate = sc.v1 ? ck.cycle_of(sc.g1) : cy_pub;
      RCY(d_sch); RD(h, s, 0, d_sch - cy_pub);
      kready = __builtin_amdgcn_readfirstlane(kfv);
      if (!kpre && s < NSEG - 2) {
        if (kready < (unsigned)s + 2u) flags_wait(cx, kfo, s + 2);
#pragma unroll
        for (int t = 0; t < 3; ++t) kx[t] = ld16(rk, kxo + t * 1024, (s + 1) * (H * 12288));
      }
    }
    RCY(d_end); RD(h, s, 1, d_det - d_rdy); RD(h, s, 2, cy_pub - d_det); RD(h, s, 3, sc.r1); RD(h, s, 4, sc.r2); RD(h, s, 5, d_end - cy_pub); RD(h, s, 6, gate - cy_pub);
    pbase = sbase;
  }
  VMWAIT(0); barx();
  if (tid == 192) flag_set(cx, C_MF1 + h * 32, 4u * NSEG);
}

constexpr int L2_IMG = 0, L2_UM = 10240, L2_UX = L2_UM + 4096, L2_W = L2_UX + 2 * 16384, L2_MISC = L2_W + 4 * 8 * 2048;

__device__ __forceinline__ void layer2(const Args& a, const int cg, const unsigned L0, Ctx& cx) {
  const int tid = threadIdx.x, lane = tid & 63, w = __builtin_amdgcn_readfirstlane(tid >> 6);
  const rsrc_t rx = mkrs(a.ws + O_XR, (size_t)NSEG * XSTEP), rum = mkrs(a.ws + O_UM, (size_t)NSEG * 80 * 4096);
  const unsigned wl = L0 + L2_W + w * 8 * 2048 + lane * 16;
  v8u wr[8][4];
  {
    const v4u* src = (const v4u*)(a.ws + O_BT + ((size_t)(cg * 4 + w) * 40) * 2048) + lane;
#pragma unroll
    for (int f = 0; f < 40; ++f) {
      const v4u lo = src[f * 128], hi = src[f * 128 + 64];
      if (f >= 32) { ldsst<v4u>(wl + (f - 32) * 2048, lo); ldsst<v4u>(wl + (f - 32) * 2048 + 1024, hi); }
      else wr[f >> 2][f & 3] = cat8(lo, hi);
    }
  }
  const unsigned long long selmask = __ballot(((const unsigned*)(a.ws + O_SEL))[lane] != 0u);
  const unsigned sel_lo = sel_smooth(__builtin_amdgcn_readfirstlane((unsigned)selmask)), sel_hi = sel_smooth(__builtin_amdgcn_readfirstlane((unsigned)(selmask >> 32)));
  const int sidx = (lane & 8) ? 0xEEEE : 0x4444;
  unsigned moff[4], po[10];
#pragma unroll
  for (int k = 0; k < 4; ++k) { int b = w + 3 * k; if (b >= 10) b = w; moff[k] = xmap(b * 1024 + lane * 16); }
#pragma unroll
  for (int u = 0; u < 10; ++u) po[u] = xmap(XP + (w & 1) * 10240 + u * 1024 + lane * 16);
  const unsigned mst0 = L0 + L2_IMG + w * 1024 + lane * 16, mst3 = L0 + L2_IMG + (w == 0 ? 9 : w) * 1024 + lane * 16;
  const unsigned umo = L0 + L2_UM + ((w >> 1) * 2 + (lane & 1)) * 1024 + ((2 * (w & 1)) * 16 + 4 * ((lane & 7) >> 1) + 2 * (lane >> 5) + ((lane >> 3) & 1)) * 16 + ((lane >> 4) & 1) * 8;
  const unsigned ooff = xmap(cg * 128 + (4 * (w & 1) + 2 * ((lane >> 3) & 1) + (lane & 1)) * 16 + (lane >> 4) * 4);
  const unsigned t1o = xmap(XT1 + (lane & 31) * 16), t2o0 = xmap(XT2 + lane * 16), t2o1 = xmap(XT2 + 1024 + (lane & 15) * 16);
  const unsigned ufo = (C_UF + (lane & 31) * 4) * 4;
  auto ux_dma = [&](int s) {
    const char* up = a.ws + O_UX + ((size_t)s * 80 + cg) * 16384 + lane * 16;
#pragma unroll
    for (int v = 0; v < 16; ++v) dma16(up + v * 1024, L0 + L2_UX + (s & 1) * 16384 + v * 1024);
  };
  STAMP(cx.slot, 1);
  if (w == 3) { flags_wait(cx, ufo, 1u); ux_dma(0); }
  unsigned ufv = 0;
  unsigned tref = cy32();
  Sched sc; sc.init(a.w1q, a.w2q);
  Clock ck; ck.init();
  unsigned pbase = 0;
  const f32x4 Z4 = {0.f, 0.f, 0.f, 0.f};
#pragma unroll 1
  for (int s = 0; s < NSEG; ++s) {
    const unsigned sbase = s * XSTEP + (((s < 32 ? sel_lo : sel_hi) >> (s & 31)) & 1u) * 8192u;
    const unsigned nbase = (s + 1) * XSTEP + (((s + 1 < 32 ? sel_lo : sel_hi) >> ((s + 1) & 31)) & 1u) * 8192u;
    f32x4 acc[4];
#pragma unroll
    for (int t = 0; t < 4; ++t) { acc[t] = Z4; asm volatile("" : "+v"(acc[t])); }
    Stamps st2 = {0u, 0u};
    if (w < 2 && s > 0) ck.sync();
    if (w < 3) {
      if (s > 0) wait_cy(tref + a.w1q * 6 + a.l2d);
      v4u mv[4];
      poll<4>(rx, moff, sbase, mv, cx);
      ldsst<v4u>(mst0, mv[0]); ldsst<v4u>(mst0 + 3072, mv[1]); ldsst<v4u>(mst0 + 6144, mv[2]); ldsst<v4u>(mst3, mv[3]);
    }
    barx();
    v4u am[10];
#pragma unroll
    for (int T = 0; T < 10; ++T) am[T] = ldsld<v4u>(L0 + L2_IMG + T * 1024 + lane * 16);
    if (w == 1 && s > 0) { st2.t0 = ld4(rx, t2o0, sbase); st2.t1 = ld4(rx, lane >= 32 ? t1o + pbase : t2o1 + sbase, 0); }
    bool early = false;
    if (w == 3) {
      VMWAIT(0);
      if (s > 0 && lane == 0) flag_set(cx, C_MF2 + cg * 4, s);
      early = s + 1 < NSEG - 1 && !__any(ufv < (unsigned)s + 2u);
      if (early) ux_dma(s + 1);
    }
    SB();
    v4u lw[16];
#pragma unroll
    for (int T = 0; T < 10; ++T)
#pragma unroll
      for (int t = 0; t < 4; ++t) {
        const int i = T * 4 + t;
        acc[t] = smf(am[T], T < 8 ? wr[T < 8 ? T : 0][t] : cat8(lw[2 * (i & 7)], lw[2 * (i & 7) + 1]), acc[t], sidx);
        SB();
        if (i >= 8 && i < 24) { lw[i - 8] = ldsld<v4u>(wl + (i - 8) * 1024); SB(); }
      }
#pragma unroll
    for (int tp = 0; tp < 2; ++tp) ldsst<v2u>(umo + tp * 256, fold_pack(acc[2 * tp], acc[2 * tp + 1]));
    if (w == 1) {
      if (s > 0) {
        const unsigned ref = ck.rte * 4u;
        if (__any(st2.t0 == 0xFFFFFFFFu || st2.t1 == 0xFFFFFFFFu)) {
          sc.got_b(stamps_latest(stamps_load(rx, t1o, t1o, pbase), rx, t1o, t1o, pbase, ref, cx.dead));
          sc.got_a(stamps_latest(stamps_load(rx, t2o0, t2o1, sbase), rx, t2o0, t2o1, sbase, ref, cx.dead));
        } else {
          int am_, bm_;
          dual_max((int)(st2.t0 - ref), (int)(st2.t1 - ref), am_, bm_);
          sc.got_b(ref + (unsigned)bm_);
          sc.got_a(ref + (unsigned)am_);
        }
      }
      ldsst<v2u>(L0 + L2_MISC + 8, (v2u){ck.cycle_of(sc.g2), sc.v2 ? 1u : 0u});
    }
    pbase = sbase;
    barx();
    if (w == 3 && s + 1 < NSEG - 1 && !early) { flags_wait(cx, ufo, s + 2); ux_dma(s + 1); }
    if (s < NSEG - 1) {
      if (w < 2) {
        v4u ua[20];
        const v2u gv = ldsld<v2u>(L0 + L2_MISC + 8);
#pragma unroll
        for (int u = 0; u < 16; ++u) ua[u] = ldsld<v4u>(L0 + L2_UX + (s & 1) * 16384 + u * 1024 + lane * 16);
#pragma unroll
        for (int u = 0; u < 4; ++u) ua[16 + u] = ldsld<v4u>(L0 + L2_UM + u * 1024 + lane * 16);
#pragma unroll
        for (int u = 0; u < 20; u += 4) asm volatile("" : "+v"(ua[u]), "+v"(ua[u + 1]), "+v"(ua[u + 2]), "+v"(ua[u + 3]));
        const unsigned g2 = __builtin_amdgcn_readfirstlane(gv.x);
        SB();
        RCY(d_rdy);
        if (__builtin_amdgcn_readfirstlane(gv.y)) wait_cy(g2);
        unsigned ov;
#pragma unroll 1
        for (unsigned spins = 0;; ++spins) {
          v4u pv[10];
#pragma unroll
          for (int u = 0; u < 10; ++u) pv[u] = ld16(rx, po[u], sbase);
          SB();
          f32x4 d0 = Z4, d1 = Z4;
          unsigned m = 0;
#pragma unroll
          for (int u = 0; u < 10; ++u) {
            d0 = mf32(ua[2 * u], pv[u], d0);
            d1 = mf32(ua[2 * u + 1], pv[u], d1);
            m = max(m, umax4(pv[u]));
            SB();
          }
          ov = min(cvtpk(((d0[0] + dppf<0x102>(d0[1])) + dppf<0x104>(d0[2])) + dppf<0x106>(d0[3]),
                         ((d1[0] + dppf<0x102>(d1[1])) + dppf<0x104>(d1[2])) + dppf<0x106>(d1[3])), 0xFFFFFFFEu);
          asm volatile("" ::: "memory");
          if (!__any(m == 0xFFFFFFFFu) || cx.dead) break;
          if ((spins & 63u) == 63u) {
            if (spins > 20000u) __hip_atomic_store(cx.abortw, 1u, RLX_AGENT);
            if (__builtin_amdgcn_readfirstlane(__hip_atomic_load(cx.abortw, RLX_AGENT))) cx.dead = true;
          }
        }
        if ((lane & 6) == 0) st4(ov, rx, ooff, nbase);
        SB();
        if (w == 0) {
          const unsigned cpub = cy32();
          if (lane == 0) st4(min(ck.time_of(cpub), 0xFFFFFFFEu), rx, xmap(XT2 + cg * 16), nbase);
          ldsst<unsigned>(L0 + L2_MISC, cpub);
          RD(32 + cg, s, 0, g2 - d_rdy); RD(32 + cg, s, 1, cpub - g2);
        }
      }
      barx();
      tref = __builtin_amdgcn_readfirstlane(ldsld<unsigned>(L0 + L2_MISC));
    }
    if (w == 3) {
#pragma unroll
      for (int u = 0; u < 4; ++u) st16(ldsld<v4u>(L0 + L2_UM + u * 1024 + lane * 16), rum, cg * 4096 + u * 1024 + lane * 16, s * (80 * 4096));
      ufv = ld4(cx.rc, ufo, 0);
    }
  }
  if (w == 3) { VMWAIT(0); if (lane == 0) flag_set(cx, C_MF2 + cg * 4, NSEG); }
}

enum { R_K = 0, R_V = 1, R_Q = 2, R_F1 = 3 };
constexpr int RING = 40960, Q_PART = 3 * RING;
constexpr int K_LEAD = 6, U_LEAD = 6, V_LEAD = 9;

__device__ __forceinline__ void load_w192(const u16* W, int ldw, v4u (&wr)[20][3], unsigned L0, int w, int lane) {
  const unsigned trb = (8 * (lane >> 4) + ((lane & 15) >> 2)) * 384 + (3 * w) * 32 + (lane & 3) * 8;
  stage384((const char*)W, ldw * 2, 0, 20, L0, w, lane);
  stage384((const char*)W, ldw * 2, 160, 20, L0 + 61440, w, lane);
#pragma unroll
  for (int c = 0; c < 4; ++c) {
    if (c < 3) VMWAIT(15); else VMWAIT(0);
    barx();
#pragma unroll
    for (int ks = 0; ks < 5; ++ks)
#pragma unroll
      for (int t = 0; t < 3; ++t) { const unsigned ad = L0 + (c & 1) * 61440 + trb + ks * 32 * 384 + t * 32; wr[5 * c + ks][t] = cat(trrd(ad), trrd(ad + 4 * 384)); }
    barx();
    if (c < 2) stage384((const char*)W, ldw * 2, 160 * (c + 2), 20, L0 + (c & 1) * 61440, w, lane);
  }
}
__device__ __forceinline__ void ring_issue(const char* src, int s, unsigned L0, int w) {
  const char* p = src + (size_t)s * RING;
  const unsigned d = L0 + (s % 3) * RING + w * 10240;
  dma16x4(p, d); dma16x4(p + 4096, d + 4096); dma16x2(p + 8192, d + 8192);
}
__device__ __forceinline__ void stagger(int w) {
  for (int i = 0; i < w; ++i) asm volatile("s_nop 7\n\ts_nop 3");
}
template <int OFF> __device__ __forceinline__ void dma16o(const char* gsrc) {
  asm volatile("global_load_lds_dwordx4 %0, off offset:%1 sc1" :: "v"(gsrc), "i"(OFF) : "memory");
}
__device__ __forceinline__ void set_m0(unsigned v) { asm volatile("s_mov_b32 m0, %0\n\ts_nop 0" :: "s"(v) : "memory"); }
template <class F>
__device__ __forceinline__ void gemm192h(const v4u (&wr)[20][3], unsigned rb, f32x4 (&acc)[2][3], F&& hook) {
  const f32x4 Z4 = {0.f, 0.f, 0.f, 0.f};
#pragma unroll
  for (int mt = 0; mt < 2; ++mt)
#pragma unroll
    for (int t = 0; t < 3; ++t) acc[mt][t] = Z4;
  stagger(__builtin_amdgcn_readfirstlane(threadIdx.x >> 6));
  v4u hb[8];
#pragma unroll
  for (int i = 0; i < 8; ++i) hb[i] = ldsld<v4u>(rb + (i & 1) * 20480 + (i >> 1) * 1024);
#pragma unroll
  for (int ks = 0; ks < 20; ++ks)
#pragma unroll
    for (int mt = 0; mt < 2; ++mt) {
      const v4u hv = hb[(2 * ks + mt) & 7];
#pragma unroll
      for (int t = 0; t < 3; ++t) acc[mt][t] = mf32(wr[ks][t], hv, acc[mt][t]);
      SB();
      if (ks + 4 < 20) hb[(2 * ks + mt) & 7] = ldsld<v4u>(rb + mt * 20480 + (ks + 4) * 1024);
      hook(2 * ks + mt);
      SB();
    }
}
__device__ __forceinline__ void ring_piece(int n, const char* fsrc, unsigned fdst) {
  if ((n & 3) == 0) set_m0(fdst + (n >> 2) * 4096);
  const char* fp = fsrc + (n >> 2) * 4096;
  if ((n & 3) == 0) dma16o<0>(fp); else if ((n & 3) == 1) dma16o<1024>(fp); else if ((n & 3) == 2) dma16o<2048>(fp); else dma16o<3072>(fp);
}
__device__ __forceinline__ void gemm192(const v4u (&wr)[20][3], unsigned rb, f32x4 (&acc)[2][3], bool fill, const char* fsrc, unsigned fdst) {
  const f32x4 Z4 = {0.f, 0.f, 0.f, 0.f};
#pragma unroll
  for (int mt = 0; mt < 2; ++mt)
#pragma unroll
    for (int t = 0; t < 3; ++t) acc[mt][t] = Z4;
  stagger(__builtin_amdgcn_readfirstlane(threadIdx.x >> 6));
  v4u hb[8];
#pragma unroll
  for (int i = 0; i < 8; ++i) hb[i] = ldsld<v4u>(rb + (i & 1) * 20480 + (i >> 1) * 1024);
#pragma unroll
  for (int ks = 0; ks < 20; ++ks)
#pragma unroll
    for (int mt = 0; mt < 2; ++mt) {
      const v4u hv = hb[(2 * ks + mt) & 7];
#pragma unroll
      for (int t = 0; t < 3; ++t) acc[mt][t] = mf32(wr[ks][t], hv, acc[mt][t]);
      SB();
      if (ks + 4 < 20) hb[(2 * ks + mt) & 7] = ldsld<v4u>(rb + mt * 20480 + (ks + 4) * 1024);
      if (mt == 1 && (ks & 1) == 0 && fill) {
        if ((ks & 7) == 0) set_m0(fdst + (ks >> 3) * 4096);
        const char* fp = fsrc + (ks >> 3) * 4096;
        if ((ks & 7) == 0) dma16o<0>(fp); else if ((ks & 7) == 2) dma16o<1024>(fp); else if ((ks & 7) == 4) dma16o<2048>(fp); else dma16o<3072>(fp);
      }
      SB();
    }
}

constexpr int PU_LDS = 3 * RING, PU_MISC = PU_LDS + 32768;
__device__ __forceinline__ void pu_task(const Args& a, const int task, const unsigned L0, Ctx& cx) {
  const int tid = threadIdx.x, lane = tid & 63, w = __builtin_amdgcn_readfirstlane(tid >> 6);
  const rsrc_t rux = mkrs(a.ws + O_UX, (size_t)S * H * DM * 2), rum = mkrs(a.ws + O_UM, (size_t)NSEG * 80 * 4096), rx1 = mkrs(a.ws + O_X1, SZ_ACT);
  const rsrc_t rpx = mkrs(a.ws + O_PX, (size_t)NSEG * 65536), rpm = mkrs(a.ws + O_PM, (size_t)NSEG * 16384);
  const float* mod = (const float*)(a.ws + O_MOD);
  const unsigned P0 = L0 + PU_LDS;
  const f32x4 Z4 = {0.f, 0.f, 0.f, 0.f};
  const int s = task / 10, part = task % 10;
  const unsigned lo = (((lane & 15) >> 3) + part * 8) , li = (lane & 1) * 1024 + (4 * ((lane & 7) >> 1) + (lane >> 4)) * 16;
  const unsigned uxo = lo * 16384 + li + w * 4096, umo = lo * 4096 + li + (w >> 1) * 2048 + (w & 1) * 512;
  const unsigned usb = s * (80 * 16384);
  const int col = part * 64 + w * 16 + 4 * (lane >> 4);
  const f32x4 g = *(const f32x4*)(mod + 2 * DM + col);
  const v2u xr0 = *(const v2u*)(a.x + (size_t)(s * 32 + (lane & 15)) * DM + col), xr1 = *(const v2u*)(a.x + (size_t)(s * 32 + 16 + (lane & 15)) * DM + col);
  v4u u[2][2][4];
#define PU_LDU(buf, k2)                                                                                                     \
  _Pragma("unroll") for (int k = 0; k < 2; ++k) _Pragma("unroll") for (int nt = 0; nt < 4; ++nt)                            \
    u[buf][k][nt] = (k2) < 4 ? ld16(rux, uxo + nt * 32768 + (((k2) * 2 + k) >> 2) * 2048 + (((k2) * 2 + k) & 3) * 256, usb) \
                             : ld16(rum, umo + nt * 8192 + k * 256, s * (80 * 4096));
  v4u p[10][2];
#define PU_LDP(k0, k1)                                                                                                      \
  _Pragma("unroll") for (int k = k0; k < k1; ++k) _Pragma("unroll") for (int mt = 0; mt < 2; ++mt)                          \
    p[k][mt] = k < 8 ? ld16(rpx, (mt * 32 + 8 * w + k) * 1024 + lane * 16, s * 65536) : ld16(rpm, (mt * 8 + 2 * w + k - 8) * 1024 + lane * 16, s * 16384);
  PU_LDP(0, 4)
  flags_wait(cx, lane < 8 ? (C_UF + (8 * w + lane) * 4) * 4 : (C_MF2 + (part * 8 + (lane & 7)) * 4) * 4, s + 1);
  PU_LDU(0, 0) PU_LDU(1, 1) PU_LDP(4, 10)
#undef PU_LDP
  SB();
  f32x4 d[2][4];
#pragma unroll
  for (int mt = 0; mt < 2; ++mt)
#pragma unroll
    for (int nt = 0; nt < 4; ++nt) d[mt][nt] = Z4;
#define PU_MM(buf, k2)                                                                                                      \
  _Pragma("unroll") for (int k = 0; k < 2; ++k) _Pragma("unroll") for (int nt = 0; nt < 4; ++nt) {                          \
    d[0][nt] = mf32(u[buf][k][nt], p[(k2) * 2 + k][0], d[0][nt]); d[1][nt] = mf32(u[buf][k][nt], p[(k2) * 2 + k][1], d[1][nt]); } SB();
  PU_MM(0, 0) PU_LDU(0, 2) SB();
  PU_MM(1, 1) PU_LDU(1, 3) SB();
  PU_MM(0, 2) PU_LDU(0, 4) SB();
  PU_MM(1, 3)
  PU_MM(0, 4)
#undef PU_LDU
#undef PU_MM
#pragma unroll
  for (int mt = 0; mt < 2; ++mt)
#pragma unroll
    for (int nt = 0; nt < 4; ++nt) ldsst<f32x4>(P0 + ((w * 4 + nt) * 2 + mt) * 1024 + lane * 16, d[mt][nt]);
  barx();
#pragma unroll
  for (int mt = 0; mt < 2; ++mt) {
    const unsigned pa = P0 + (w * 2 + mt) * 1024 + lane * 16;
    const f32x4 t = (ldsld<f32x4>(pa) + ldsld<f32x4>(pa + 8192)) + (ldsld<f32x4>(pa + 16384) + ldsld<f32x4>(pa + 24576));
    const v2u xr = mt ? xr1 : xr0;
    st8((v2u){cvtpk(blo(xr.x) + rbf(g[0] * rbf(t[0])), bhi(xr.x) + rbf(g[1] * rbf(t[1]))),
              cvtpk(blo(xr.y) + rbf(g[2] * rbf(t[2])), bhi(xr.y) + rbf(g[3] * rbf(t[3])))}, rx1, ((s * 32 + mt * 16 + (lane & 15)) * DM + col) * 2, 0);
  }
  VMWAIT(0);
  if (lane == 0) __hip_atomic_fetch_add((gu32*)(a.ctl) + C_XC + s * 32, 1u, RLX_AGENT);
  if (part >= 8) {
    flags_wait(cx, (C_XC + s * 32) * 4, 40u);
    ln_segment<true, 1>(a.ws + O_X1 + (size_t)s * 32 * DM * 2, a.ws + O_MODP + 2 * DM * 2, mkrs(a.ws + O_H2X, SZ_ACT), s * RING, (part - 8) * 16, w, lane);
    VMWAIT(0);
    if (lane == 0) __hip_atomic_fetch_add((gu32*)(a.ctl) + C_HG2 + s * 4, 1u, RLX_AGENT);
  }
  barx();
}
__device__ __forceinline__ void pu_due(const Args& a, const unsigned L0, Ctx& cx, int s, int lead) {
#pragma unroll 1
  while (cx.seg < NSEG * 10 && cx.seg / 10 + lead <= s) {
    if (threadIdx.x < 64) flags_wait(cx, (C_HF2 + (cx.seg / 10) * 32) * 4, 32u);
    barx();
    pu_task(a, cx.seg, L0, cx);
    cx.seg += 96;
  }
}
__device__ __forceinline__ bool is_ahead(unsigned mnow, int s, int lead, const unsigned L0) {
  if (threadIdx.x == 0) ldsst<unsigned>(L0 + PU_MISC + 16, mnow);
  barx();
  return (int)(__builtin_amdgcn_readfirstlane(ldsld<unsigned>(L0 + PU_MISC + 16)) >> 2) + lead <= s;
}

template <int ROLE>
__device__ __forceinline__ void stream_kv(const Args& a, const int u, const unsigned L0, Ctx& cx) {
  const int tid = threadIdx.x, lane = tid & 63, w = __builtin_amdgcn_readfirstlane(tid >> 6);
  const int LEAD = ROLE == R_K ? a.lk : a.lv;
  v4u wr[20][3];
  load_w192((ROLE == R_K ? a.Wk : a.Wv) + u * DH, HD, wr, L0, w, lane);
  const char* src = a.ws + O_HX + w * 10240 + lane * 16;
  const rsrc_t rdst = mkrs(a.ws + (ROLE == R_K ? O_KX : O_VX), (size_t)S * HD * 2);
  const unsigned fword = (ROLE == R_K ? C_KF : C_VF) + u * 32;
  STAMP(cx.slot, 1);
  int known = 0;
  seg_wait<C_HF1>(cx, 0, known, lane); ring_issue(src, 0, L0, w);
  seg_wait<C_HF1>(cx, 1, known, lane); ring_issue(src, 1, L0, w);
  bool drained = true;
#pragma unroll 1
  for (int s = 0; s < NSEG; ++s) {
    if (s == 0) VMWAIT(10);
    else if (drained || s == NSEG - 1) VMWAIT(0);
    else if (ROLE == R_K) VMWAIT(13);
    else VMWAIT(14);
    barx();
    if (!drained && tid == 0) flag_set(cx, fword, s == NSEG - 1 ? s : s - 1);
    const bool ahead = cx.seg < NSEG * 10 && cx.seg / 10 + LEAD <= s;
    if (s + 2 < NSEG) seg_wait<C_HF1>(cx, s + 2, known, lane);
    f32x4 acc[2][3];
    gemm192(wr, L0 + (s % 3) * RING + lane * 16, acc, s + 2 < NSEG, src + (size_t)(s + 2) * RING, L0 + ((s + 2) % 3) * RING + w * 10240);
    const unsigned sb = s * (H * 12288);
    if (ROLE == R_K) {
      const unsigned o = (u * 4 + w) * 3072 + lane * 16;
      st16(cat(pack4(acc[0][0]), pack4(acc[0][1])), rdst, o, sb);
      st16(cat(pack4(acc[1][0]), pack4(acc[1][1])), rdst, o + 1024, sb);
      st16(cat(pack4(acc[0][2]), pack4(acc[1][2])), rdst, o + 2048, sb);
    } else {
      const unsigned o = u * 12288 + lane * 16;
#pragma unroll
      for (int mt = 0; mt < 2; ++mt) {
        st16(cat(pack4(acc[mt][0]), pack4(acc[mt][1])), rdst, o + mt * 6144 + w * 1024, sb);
        st8(pack4(acc[mt][2]), rdst, o + mt * 6144 + (4 + (w >> 1)) * 1024 + (w & 1) * 8, sb);
      }
    }
    drained = s < 2 || s == NSEG - 1 || ahead;
    if (drained) {
      VMWAIT(0); barx();
      if (tid == 0) flag_set(cx, fword, s + 1);
    }
    STAMP(cx.slot, 8 + s);
    if (ahead) pu_due(a, L0, cx, s, LEAD);
  }
}

__device__ __forceinline__ void stream_f1(const Args& a, const int u, const unsigned L0, Ctx& cx) {
  const int tid = threadIdx.x, lane = tid & 63, w = __builtin_amdgcn_readfirstlane(tid >> 6);
  v4u wr[20][3];
  load_w192(a.w1 + u * DH, FF, wr, L0, w, lane);
  const char* src = a.ws + O_H2X + w * 10240 + lane * 16;
  const char* hfp = a.ctl + (C_HG2 + lane * 4) * 4;
  const rsrc_t rdst = mkrs(a.ws + O_FX, (size_t)S * FF * 2);
  float bias[3][4];
#pragma unroll
  for (int t = 0; t < 3; ++t)
#pragma unroll
    for (int r = 0; r < 4; ++r) bias[t][r] = bf2f(a.b1[u * DH + 16 * (3 * w + t) + 4 * (lane >> 4) + r]);
  STAMP(cx.slot, 1);
  int issued = 0, known = 0;
  bool owe = false;
#pragma unroll 1
  for (int s = 0; s < NSEG; ++s) {
    if (issued == s) { seg_wait<C_HG2>(cx, s, known, lane); ring_issue(src, s, L0, w); issued = s + 1; VMWAIT(0); }
    else if (owe) VMWAIT(4);
    barx();
    const bool fill = issued < known && issued <= s + 2;
    unsigned fv = ld4_async(hfp);
    f32x4 acc[2][3];
    gemm192(wr, L0 + (s % 3) * RING + lane * 16, acc, fill, src + (size_t)issued * RING, L0 + (issued % 3) * RING + w * 10240);
    if (fill) ++issued;
#pragma unroll
    for (int mt = 0; mt < 2; ++mt)
#pragma unroll
      for (int t = 0; t < 3; ++t)
#pragma unroll
        for (int r = 0; r < 4; r += 2) {
          const unsigned p = cvtpk(acc[mt][t][r], acc[mt][t][r + 1]);
          const unsigned q = cvtpk(blo(p) + bias[t][r], bhi(p) + bias[t][r + 1]);
          const float v0 = __builtin_amdgcn_fmed3f(blo(q), 0.f, 3.4e38f), v1 = __builtin_amdgcn_fmed3f(bhi(q), 0.f, 3.4e38f);
          acc[mt][t][r] = v0 * v0; acc[mt][t][r + 1] = v1 * v1;
        }
    if (fill) VMWAIT(10); else VMWAIT(0);
    PIN(fv);
    if (owe && lane == 0) __hip_atomic_fetch_add((gu32*)(a.ctl) + C_FF + u * 4, 1u, RLX_AGENT);
    { const unsigned long long miss = __ballot(fv < 8u); known = miss ? __builtin_ctzll(miss) : 64; }
    if (issued == s + 1 && issued < known) { ring_issue(src, issued, L0, w); ++issued; }
    const unsigned o = u * 12288 + lane * 16, sb = s * (8 * 12288);
#pragma unroll
    for (int mt = 0; mt < 2; ++mt) {
      st16(cat(pack4(acc[mt][0]), pack4(acc[mt][1])), rdst, o + mt * 6144 + w * 1024, sb);
      st8(pack4(acc[mt][2]), rdst, o + mt * 6144 + (4 + (w >> 1)) * 1024 + (w & 1) * 8, sb);
    }
    owe = issued > s + 1 && known > s + 2;
    if (!owe) {
      VMWAIT(0);
      if (lane == 0) __hip_atomic_fetch_add((gu32*)(a.ctl) + C_FF + u * 4, 1u, RLX_AGENT);
    }
    STAMP(cx.slot, 8 + s);
  }
}

__device__ __forceinline__ void stream_q(const Args& a, const int u, const unsigned L0, Ctx& cx) {
  const int tid = threadIdx.x, lane = tid & 63, w = __builtin_amdgcn_readfirstlane(tid >> 6);
  v4u wr[20][3];
  load_w192(a.Wq + u * DH, HD, wr, L0, w, lane);
  const char* src = a.ws + O_HX + w * 10240 + lane * 16;
  const rsrc_t rpx = mkrs(a.ws + O_PX, (size_t)NSEG * 65536), rpm = mkrs(a.ws + O_PM, (size_t)NSEG * 16384);
  const rsrc_t rk = mkrs(a.ws + O_KX, (size_t)S * HD * 2), rkm = mkrs(a.ws + O_KM, (size_t)NSEG * H * 6144);
  const unsigned kfo = (C_KF + u * 32) * 4, mfo = (C_MF1 + u * 32) * 4, ko = (u * 4 + w) * 3072 + lane * 16, mo = (u * 4 + w) * 1536;
  const unsigned fo = (lane & 1) ? kfo : mfo;
  const int smt = w >> 1, g = lane & 7, c16 = 8 * (w & 1) + (lane >> 3), last = 16 * smt + c16 - 4 * g;
  const unsigned pbuf = L0 + Q_PART;
  const unsigned rxa = pbuf + (smt * 3 + (g >> 2)) * 1024 + ((g & 3) * 16 + c16) * 16;
  const unsigned rma = pbuf + (smt * 3 + 2) * 1024 + ((2 + (g >> 2)) * 16 + c16) * 16 + (g & 3) * 4;
  const unsigned sxo = (smt * 32 + u) * 1024 + ((g & 3) * 16 + c16) * 16 + (g >> 2) * 8;
  const unsigned smo = (smt * 8 + (u >> 2)) * 1024 + ((u & 3) * 16 + c16) * 16 + g * 2;
  const f32x4 Z4 = {0.f, 0.f, 0.f, 0.f};
  STAMP(cx.slot, 1);
  int known = 0;
  seg_wait<C_HF1>(cx, 2, known, lane);
  ring_issue(src, 0, L0, w); ring_issue(src, 1, L0, w);
  v4u q01[2]; v2u q2[2];
  unsigned kready = 0, mready = 0, fv = 0;
  int owe = -1;
  bool fast = false;
#pragma unroll 1
  for (int s = -1; s < NSEG; ++s) {
    v4u k0, k1, k2, m01; v2u m2;
    f32x4 t0, t1, t2, t3, t4, xa, xb, xc, xd;
    float ma, mb, mc, md, v0, v1, v2, v3, v4, mx, e0, e1, e2, e3, e4, sum, inv;
    unsigned mr0, a0, a1, a2;
    const unsigned pa = pbuf + w * 6144 + lane * 16;
    auto att = [&](int i) {
      const float NEG = -3.0e38f, BIG = 3.4e38f;
      if (i == 12) { t0 = mf32(k0, q01[0], Z4); t1 = mf32(k0, q01[1], Z4); t2 = mf32(k1, q01[1], Z4); }
      if (i == 13) { t3 = mf32(m01, q01[0], Z4); t4 = mf32(m01, q01[1], Z4); }
      if (i == 14) { t0 = mf32(k2, (v4u){q2[0].x, q2[0].y, 0u, 0u}, t0); t1 = mf32(k2, (v4u){q2[1].x, q2[1].y, 0u, 0u}, t1); t2 = mf32(k2, (v4u){0u, 0u, q2[1].x, q2[1].y}, t2); }
      if (i == 15) { const v4u mz = {m2.x, m2.y, 0u, 0u}; t3 = mf32(mz, (v4u){q2[0].x, q2[0].y, 0u, 0u}, t3); t4 = mf32(mz, (v4u){q2[1].x, q2[1].y, 0u, 0u}, t4); }
      if (i == 17) { ldsst<f32x4>(pa, t0); ldsst<f32x4>(pa + 3072, t1); ldsst<f32x4>(pa + 4096, t2); }
      if (i == 18) { ldsst<f32x4>(pa + 2048, t3); ldsst<f32x4>(pa + 5120, t4); if (tid == 0) ldsst<unsigned>(pbuf + 24576, mready); }
      if (i == 20) {
        barx();
        if (owe >= 0 && tid == 0) __hip_atomic_fetch_add((gu32*)a.ctl + C_HF2 + owe * 32, 1u, RLX_AGENT);
        owe = -1;
        mr0 = ldsld<unsigned>(pbuf + 24576);
        xa = ldsld<f32x4>(rxa); xb = ldsld<f32x4>(rxa + 6144); xc = ldsld<f32x4>(rxa + 12288); xd = ldsld<f32x4>(rxa + 18432);
        ma = ldsld<float>(rma); mb = ldsld<float>(rma + 6144); mc = ldsld<float>(rma + 12288); md = ldsld<float>(rma + 18432);
      }
      if (i == 23) { xa = (xa + xb) + (xc + xd); ma = (ma + mb) + (mc + md); }
      if (i == 24) { a0 = cvtpk(xa[0], xa[1]); a1 = cvtpk(xa[2], xa[3]); a2 = cvtpk(ma, ma); a0 = cvtpk(blo(a0) * SCALE, bhi(a0) * SCALE); }
      if (i == 25) { a1 = cvtpk(blo(a1) * SCALE, bhi(a1) * SCALE); a2 = cvtpk(blo(a2) * SCALE, 0.f); }
      if (i == 26) { v0 = last >= 0 ? blo(a0) : NEG; v1 = last >= 1 ? bhi(a0) : NEG; v2 = last >= 2 ? blo(a1) : NEG; v3 = last >= 3 ? bhi(a1) : NEG; v4 = blo(a2); }
      if (i == 27) { mx = __builtin_amdgcn_fmed3f(__builtin_amdgcn_fmed3f(v0, v1, BIG), __builtin_amdgcn_fmed3f(v2, v3, BIG), BIG); mx = __builtin_amdgcn_fmed3f(mx, v4, BIG);
                     mx = __builtin_amdgcn_fmed3f(mx, dppf<0xB1>(mx), BIG); }
      if (i == 28) { mx = __builtin_amdgcn_fmed3f(mx, dppf<0x4E>(mx), BIG); mx = __builtin_amdgcn_fmed3f(mx, dppf<0x141>(mx), BIG); }
      if (i == 29) { e0 = __builtin_amdgcn_exp2f((v0 - mx) * LOG2E); e1 = __builtin_amdgcn_exp2f((v1 - mx) * LOG2E); }
      if (i == 30) { e2 = __builtin_amdgcn_exp2f((v2 - mx) * LOG2E); e3 = __builtin_amdgcn_exp2f((v3 - mx) * LOG2E); }
      if (i == 31) { e4 = __builtin_amdgcn_exp2f((v4 - mx) * LOG2E); sum = ((e0 + e1) + (e2 + e3)) + e4; }
      if (i == 32) { sum += dppf<0xB1>(sum); sum += dppf<0x4E>(sum); sum += dppf<0x141>(sum); inv = __builtin_amdgcn_rcpf(sum); }
      if (i == 34) st8((v2u){cvtpk(e0 * inv, e1 * inv), cvtpk(e2 * inv, e3 * inv)}, rpx, sxo, s * 65536);
      if (i == 36) { const float pm = e4 * inv, po = dppf<0xB1>(pm); if (!(g & 1)) st4(cvtpk(pm, po), rpm, smo, s * 16384); }
    };
    if (s >= 0) {
      if (kready < (unsigned)s + 1u) flags_wait(cx, kfo, s + 1);
      k0 = ld16(rk, ko, s * (H * 12288)); k1 = ld16(rk, ko + 1024, s * (H * 12288)); k2 = ld16(rk, ko + 2048, s * (H * 12288));
    }
    const bool fill = s + 3 < NSEG;
    const char* fsrc = src + (size_t)(s + 3) * RING;
    const unsigned fdst = L0 + ((s + 3) % 3) * RING + w * 10240;
    v4u n01[2]; v2u n2[2];
    if (s + 1 < NSEG) {
      if (s < 0) VMWAIT(10);
      barx();
      if (fill) seg_wait<C_HF1>(cx, s + 3, known, lane);
      fv = ld4_async(a.ctl + fo);
      f32x4 acc[2][3];
      if (fast) {
        m01 = ld16(rkm, mo + lane * 16, s * (H * 6144)); m2 = ld8(rkm, mo + 1024 + lane * 8, s * (H * 6144));
        gemm192h(wr, L0 + ((s + 1) % 3) * RING + lane * 16, acc, [&](int i) {
          if (i == 11) { VMWAIT(0); PIN(fv); mready = __builtin_amdgcn_readlane(fv, 0); kready = __builtin_amdgcn_readlane(fv, 1); }
          att(i);
          if (i >= 21 && (i & 1) && fill) ring_piece((i - 21) >> 1, fsrc, fdst);
        });
      } else {
        gemm192(wr, L0 + ((s + 1) % 3) * RING + lane * 16, acc, fill, fsrc, fdst);
      }
#pragma unroll
      for (int mt = 0; mt < 2; ++mt) { n01[mt] = cat(pack4(acc[mt][0]), pack4(acc[mt][1])); n2[mt] = pack4(acc[mt][2]); }
    }
    if (!fast) {
      if (fill) VMWAIT(10); else VMWAIT(0);
      PIN(fv);
      mready = __builtin_amdgcn_readlane(fv, 0); kready = __builtin_amdgcn_readlane(fv, 1);
      if (s >= 0) {
        if (mready < 4u * (s + 1)) flags_wait(cx, mfo, 4 * (s + 1));
        m01 = ld16(rkm, mo + lane * 16, s * (H * 6144)); m2 = ld8(rkm, mo + 1024 + lane * 8, s * (H * 6144));
#pragma unroll
        for (int i = 12; i < 37; ++i) att(i);
      }
    }
    if (s >= 0) {
      fast = __builtin_amdgcn_readfirstlane(mr0) >= 4u * (s + 2) && s + 2 < NSEG;
      if (fast) owe = s;
      else {
        fv = ld4_async(a.ctl + fo);
        VMWAIT(0);
        PIN(fv);
        if (tid == 0) ldsst<unsigned>(pbuf + 24580, fv);
        barx();
        if (tid == 0) __hip_atomic_fetch_add((gu32*)a.ctl + C_HF2 + s * 32, 1u, RLX_AGENT);
        fast = __builtin_amdgcn_readfirstlane(ldsld<unsigned>(pbuf + 24580)) >= 4u * (s + 2) && s + 2 < NSEG;
      }
      STAMP(cx.slot, 8 + s);
    }
#pragma unroll
    for (int mt = 0; mt < 2; ++mt) { q01[mt] = n01[mt]; q2[mt] = n2[mt]; }
  }
}

__device__ __forceinline__ void unit_u(const Args& a, const int h, const unsigned L0, Ctx& cx) {
  const int tid = threadIdx.x, lane = tid & 63, w = __builtin_amdgcn_readfirstlane(tid >> 6);
  v2u wh[12][10];
  {
    const char* W = (const char*)(a.Wo + (size_t)h * DH * DM);
    const unsigned trb = (4 * (lane >> 4) + ((lane & 15) >> 2)) * 1280 + w * 320 + (lane & 3) * 8;
    auto stage = [&](int c) {
#pragma unroll 1
      for (int i = w; i < 60; i += 4) dma16p(W + c * 61440 + i * 1024 + lane * 16, L0 + (c & 1) * 61440 + i * 1024);
    };
    stage(0); stage(1);
#pragma unroll
    for (int c = 0; c < 4; ++c) {
      if (c < 3) VMWAIT(15); else VMWAIT(0);
      barx();
#pragma unroll
      for (int t = 0; t < 3; ++t)
#pragma unroll
        for (int nt = 0; nt < 10; ++nt) wh[3 * c + t][nt] = trrd(L0 + (c & 1) * 61440 + trb + t * 16 * 1280 + nt * 32);
      barx();
      if (c < 2) stage(c + 2);
    }
  }
  const char* src = a.ws + O_VX + (size_t)h * 12288 + w * 3072 + lane * 16;
  const rsrc_t rdst = mkrs(a.ws + O_UX, (size_t)S * H * DM * 2);
  const unsigned vfo = (C_VF + h * 32) * 4;
  const unsigned so = ((lane & 15) >> 3) * 16384 + (h >> 2) * 2048 + (lane & 1) * 1024 + ((h & 3) * 16 + 4 * ((lane & 7) >> 1) + (lane >> 4)) * 16 + w * (20 * 16384);
  auto issue = [&](int s) {
    dma16x2(src + (size_t)s * (H * 12288), L0 + (s & 3) * 12288 + w * 3072);
    dma16(src + (size_t)s * (H * 12288) + 2048, L0 + (s & 3) * 12288 + w * 3072 + 2048);
  };
  STAMP(cx.slot, 1);
  int issued = 0;
  bool drained = true;
  const f32x4 Z4 = {0.f, 0.f, 0.f, 0.f};
#pragma unroll 1
  for (int s = 0; s < NSEG; ++s) {
    if (issued == s) { flags_wait(cx, vfo, s + 1); issue(s); issued = s + 1; VMWAIT(0); }
    else if (drained) VMWAIT(0);
    else VMWAIT(5);
    barx();
    if (!drained && tid == 0) flag_set(cx, C_UF + h * 4, s - 1);
    const bool ahead = cx.seg < NSEG * 10 && cx.seg / 10 + a.lu <= s;
    const unsigned rb = L0 + (s & 3) * 12288 + lane * 16;
    v4u vf[2][6];
#pragma unroll
    for (int mt = 0; mt < 2; ++mt)
#pragma unroll
      for (int ks = 0; ks < 6; ++ks) vf[mt][ks] = ldsld<v4u>(rb + (mt * 6 + ks) * 1024);
    const unsigned vnow = ld4(cx.rc, vfo, 0);
#pragma unroll
    for (int nt = 0; nt < 10; ++nt) {
      f32x4 d0 = Z4, d1 = Z4;
#pragma unroll
      for (int ks = 0; ks < 6; ++ks) {
        const v4u b = cat(wh[ks < 4 ? 3 * ks : (ks == 4 ? 2 : 8)][nt], wh[ks < 4 ? 3 * ks + 1 : (ks == 4 ? 5 : 11)][nt]);
        d0 = mf32(vf[0][ks], b, d0);
        d1 = mf32(vf[1][ks], b, d1);
      }
      st16(cat(pack4(d0), pack4(d1)), rdst, so + nt * 32768, s * (80 * 16384));
      if (nt == 4 && s + 1 < NSEG && (unsigned)__builtin_amdgcn_readfirstlane(vnow) >= (unsigned)s + 2u) { issue(s + 1); issued = s + 2; }
    }
    drained = s < 2 || s == NSEG - 1 || ahead;
    if (drained) {
      VMWAIT(0); barx();
      if (tid == 0) flag_set(cx, C_UF + h * 4, s + 1);
    }
    STAMP(cx.slot, 8 + s);
    if (ahead) pu_due(a, L0, cx, s, a.lu);
  }
}

__device__ __forceinline__ void unit_f2(const Args& a, const int jj, const unsigned L0, Ctx& cx) {
  const int tid = threadIdx.x, lane = tid & 63, w = __builtin_amdgcn_readfirstlane(tid >> 6);
  v2u wh[2][12][5];
  {
    const unsigned trb = w * 30720 + (4 * (lane >> 4) + ((lane & 15) >> 2)) * 160 + (lane & 3) * 8;
#pragma unroll
    for (int c = 0; c < 2; ++c) {
      for (int i = 0; i < 30; ++i) {
        const unsigned idx = i * 64 + lane, row = idx / 10, ch = idx % 10;
        dma16p(a.w2 + (size_t)(384 * w + 192 * c + row) * DM + jj * 80 + ch * 8, L0 + w * 30720 + i * 1024);
      }
      VMWAIT(0); barx();
#pragma unroll
      for (int t = 0; t < 12; ++t)
#pragma unroll
        for (int nt = 0; nt < 5; ++nt) wh[c][t][nt] = trrd(L0 + trb + t * 16 * 160 + nt * 32);
      barx();
    }
  }
  const rsrc_t rf = mkrs(a.ws + O_FX, (size_t)S * FF * 2), rx1 = mkrs(a.ws + O_X1, SZ_ACT);
  const unsigned ffo = (C_FF + (2 * w + (lane & 1)) * 4) * 4, fo = 2 * w * 12288 + lane * 16;
  v2u bb[3]; f32x4 gg[3]; unsigned oo[3];
#pragma unroll
  for (int k = 0; k < 3; ++k) {
    const int i = w + 4 * k < 10 ? w + 4 * k : w, col = jj * 80 + (i % 5) * 16 + 4 * (lane >> 4);
    bb[k] = *(const v2u*)(a.b2 + col);
    gg[k] = *(const f32x4*)((const float*)(a.ws + O_MOD) + 5 * DM + col);
    oo[k] = (((i / 5) * 16 + (lane & 15)) * DM + col) * 2;
  }
  STAMP(cx.slot, 1);
  const f32x4 Z4 = {0.f, 0.f, 0.f, 0.f};
  v4u fa[12], fb[12];
  bool have = false;
#pragma unroll 1
  for (int s = 0; s < NSEG; ++s) {
    const unsigned sb = s * (8 * 12288);
    if (!have) {
      flags_wait(cx, ffo, 4 * (s + 1));
#pragma unroll
      for (int i = 0; i < 12; ++i) fa[i] = ld16(rf, fo + i * 1024, sb);
    }
    const unsigned nf = ld4(cx.rc, ffo, 0);
    v2u xr[3];
    f32x4 acc[2][5];
#pragma unroll
    for (int mt = 0; mt < 2; ++mt)
#pragma unroll
      for (int nt = 0; nt < 5; ++nt) acc[mt][nt] = Z4;
    SB();
#pragma unroll
    for (int ks = 0; ks < 6; ++ks)
#pragma unroll
      for (int nt = 0; nt < 5; ++nt) {
        const v4u wa = cat(wh[0][ks < 4 ? 3 * ks : (ks == 4 ? 2 : 8)][nt], wh[0][ks < 4 ? 3 * ks + 1 : (ks == 4 ? 5 : 11)][nt]);
        acc[0][nt] = mf32(wa, fa[ks], acc[0][nt]);
        acc[1][nt] = mf32(wa, fa[6 + ks], acc[1][nt]);
        SB();
        if (nt == 0) fb[ks] = ld16(rf, fo + 12288 + ks * 1024, sb);
        if (nt == 2) fb[6 + ks] = ld16(rf, fo + 12288 + (6 + ks) * 1024, sb);
        if (nt == 4 && ks < 3) xr[ks] = ld8(rx1, oo[ks], s * (32 * DM * 2));
        SB();
      }
    have = s + 1 < NSEG && !__any(nf < 4u * (s + 2));
    const unsigned nsb = have ? sb + 8 * 12288 : sb;
    SB();
#pragma unroll
    for (int ks = 0; ks < 6; ++ks)
#pragma unroll
      for (int nt = 0; nt < 5; ++nt) {
        const v4u wa = cat(wh[1][ks < 4 ? 3 * ks : (ks == 4 ? 2 : 8)][nt], wh[1][ks < 4 ? 3 * ks + 1 : (ks == 4 ? 5 : 11)][nt]);
        acc[0][nt] = mf32(wa, fb[ks], acc[0][nt]);
        acc[1][nt] = mf32(wa, fb[6 + ks], acc[1][nt]);
        SB();
        if (nt == 1) fa[ks] = ld16(rf, fo + ks * 1024, nsb);
        if (nt == 3) fa[6 + ks] = ld16(rf, fo + (6 + ks) * 1024, nsb);
        SB();
      }
    const unsigned pb = L0 + (s & 1) * 40960;
#pragma unroll
    for (int mt = 0; mt < 2; ++mt)
#pragma unroll
      for (int nt = 0; nt < 5; ++nt) ldsst<f32x4>(pb + (w * 10 + mt * 5 + nt) * 1024 + lane * 16, acc[mt][nt]);
    barx();
#pragma unroll
    for (int k = 0; k < 3; ++k) {
      if (w + 4 * k < 10) {
        const unsigned pa = pb + (w + 4 * k) * 1024 + lane * 16;
        const f32x4 t = (ldsld<f32x4>(pa) + ldsld<f32x4>(pa + 10240)) + (ldsld<f32x4>(pa + 20480) + ldsld<f32x4>(pa + 30720));
        const float o0 = blo(xr[k].x) + rbf(gg[k][0] * rbf(rbf(t[0]) + blo(bb[k].x))), o1 = bhi(xr[k].x) + rbf(gg[k][1] * rbf(rbf(t[1]) + bhi(bb[k].x)));
        const float o2 = blo(xr[k].y) + rbf(gg[k][2] * rbf(rbf(t[2]) + blo(bb[k].y))), o3 = bhi(xr[k].y) + rbf(gg[k][3] * rbf(rbf(t[3]) + bhi(bb[k].y)));
        *(v2u*)((char*)a.out + (size_t)s * (32 * DM * 2) + oo[k]) = (v2u){cvtpk(o0, o1), cvtpk(o2, o3)};
      }
    }
    STAMP(cx.slot, 8 + s);
  }
}

__global__ void __launch_bounds__(256, 1) k_main(Args a) {
  extern __shared__ __attribute__((aligned(16))) char lds_[];
  const unsigned L0 = (unsigned)(size_t)(LAS char*)lds_;
  const int tid = threadIdx.x, lane = tid & 63, w = __builtin_amdgcn_readfirstlane(tid >> 6);
  a.ctl = a.ws + O_CTL + (__builtin_popcountll(__ballot(((const unsigned*)(a.ws + O_SEL))[NSEG + (lane & 3)] != 0u) & 7ull) >= 2 ? 0 : 8192);
  if (tid == 0) {
    unsigned x = xcc_id() & 7u, slot = 0;
    for (int tries = 0; tries < 8; ++tries) {
      const unsigned rk = __hip_atomic_fetch_add((gu32*)(a.ctl) + C_TICK + x * 32, 1u, RLX_AGENT);
      if (rk < 32u) { slot = x * 32u + rk; break; }
      x = (x + 1u) & 7u;
    }
    ldsst<unsigned>(L0, slot);
  }
  barx();
  const unsigned slot = __builtin_amdgcn_readfirstlane(ldsld<unsigned>(L0));
  barx();
  Ctx cx{mkrs(a.ctl, 65536), (gu32*)(a.ctl) + C_ABORT, false, slot, 0};
  STAMP(slot, 0);
  const unsigned x = slot >> 5, rk = slot & 31u;
  if (x < 4 && rk < 8) { layer1(a, x * 8 + rk, L0, cx); STAMP(slot, 2); STAMP(slot, 3); return; }
  if (x < 4 && rk < 28) { layer2(a, x * 20 + (rk - 8), L0, cx); STAMP(slot, 2); STAMP(slot, 3); return; }
  auto ln1 = [&](int seg) {
    ln_segment<false, 2>((const char*)(a.x + (size_t)seg * 32 * DM), a.ws + O_MODP, mkrs(a.ws + O_HX, SZ_ACT), seg * RING, 0, w, lane);
    VMWAIT(0); barx();
    if (tid == 0) flag_set(cx, C_HF1 + seg * 4, 1u);
  };
  if (x < 4) {
    const int f = x * 4 + (rk - 28);
    ln1(32 + 2 * f); ln1(33 + 2 * f);
    flags_wait(cx, C_UF * 4, 1u);
    if (f < 8) stream_f1(a, f, L0, cx); else unit_f2(a, f - 8, L0, cx);
    STAMP(slot, 2); STAMP(slot, 3);
    return;
  }
  const int h = (x - 4) * 8 + (rk & 7), kind = rk >> 3;
  if (kind == 3) { ln1(h); flags_wait(cx, (C_VF + h * 32) * 4, 1u); }
  cx.seg = h * 3 + kind;
  if (kind == 0) stream_kv<R_K>(a, h, L0, cx);
  else if (kind == 1) stream_kv<R_V>(a, h, L0, cx);
  else if (kind == 2) unit_u(a, h, L0, cx);
  else { stream_q(a, h, L0, cx); STAMP(slot, 2); STAMP(slot, 3); return; }
  STAMP(slot, 2);
  barx();
  pu_due(a, L0, cx, 1000, 0);
  STAMP(slot, 3);
}

extern "C" void kernel_launch(void* const* d_in, const int* in_sizes, int n_in, void* d_out, int out_size, void* d_ws, size_t ws_size, hipStream_t stream) {
  const size_t pad = (size_t)(-(intptr_t)d_ws) & ((2u << 20) - 1u);
  if (ws_size < WS_NEED + pad) return;
  Args a;
  a.x = (const u16*)d_in[0]; a.mem0 = (const u16*)d_in[1]; a.Wq = (const u16*)d_in[2]; a.Wk = (const u16*)d_in[3]; a.Wv = (const u16*)d_in[4]; a.Wo = (const u16*)d_in[5];
  a.w1 = (const u16*)d_in[8]; a.b1 = (const u16*)d_in[9]; a.w2 = (const u16*)d_in[10]; a.b2 = (const u16*)d_in[11]; a.c = (const u16*)d_in[12]; a.wmod = (const u16*)d_in[13];
  a.ws = (char*)d_ws + pad; a.ctl = a.ws; a.out = (u16*)d_out;
  a.w1q = 50;
  a.w2q = 62;
  a.l2d = 300; a.lk = 10; a.lu = 10; a.lv = 14;
  (void)hipFuncSetAttribute((const void*)k_main, hipFuncAttributeMaxDynamicSharedMemorySize, MAIN_LDS);
  hipLaunchKernelGGL(k_pre, dim3(256), dim3(256), 0, stream, a);
  hipLaunchKernelGGL(k_main, dim3(256), dim3(256), MAIN_LDS, stream, a);
}
```
